```python
import torch

def _softmax(x, axis=-1):
    return torch.softmax(x, dim=axis)

def _gelu(x):
    return torch.nn.functional.gelu(x, approximate='tanh')

def _silu(x):
    return torch.nn.functional.silu(x)

def _sigmoid(x):
    return torch.sigmoid(x)

def _layernorm(x, axis=-1, eps=1e-05):
    m = x.mean(dim=axis, keepdim=True)
    v = x.var(dim=axis, keepdim=True, unbiased=False)
    return (x - m) / torch.sqrt(v + eps)

def _scatter_add(tgt, idx, src, axis=0):
    return tgt.clone().index_add_(axis, idx, src)

def _segment_sum(src, seg, n):
    out = torch.zeros((n,) + src.shape[1:], dtype=src.dtype)
    return out.index_add_(0, seg, src)

def _pad(x, pw):
    flat = tuple((v for pair in reversed(pw) for v in pair))
    return torch.nn.functional.pad(x, flat)
kernel_name = 'adaLN_cond__sliding_block_cached__mixer_channel_mlp__decode'

def setup_inputs():

    _w = lambda a: (lambda t: t.to(torch.bfloat16) if t.is_floating_point() else t)(torch.as_tensor(a))

    def _A_setup():
        B, Sq, D = (2, 1, 1024)
        Hq, Hkv, Sk, Dh = (32, 4, 32768, 128)
        assert Hq % Hkv == 0, (Hq, Hkv)
        return {'x': _w((lambda *s: torch.randn(*s, dtype=torch.bfloat16) * 0.05)(B, Sq, D)), 'k_cache': _w((lambda *s: torch.randn(*s, dtype=torch.bfloat16) * 3.2)(B, Hkv, Sk, Dh)), 'v_cache': _w((lambda *s: torch.randn(*s, dtype=torch.bfloat16) * 0.4)(B, Hkv, Sk, Dh)), 'wq': _w((lambda *s: torch.randn(*s, dtype=torch.bfloat16) * 3.2)(D, Hq * Dh) * D ** (-0.5)), 'wo': _w((lambda *s: torch.randn(*s, dtype=torch.bfloat16) * 0.4)(Hq * Dh, D) * (Hq * Dh) ** (-0.5))}
    _A_in = _A_setup()

    def _B_setup():
        B, S, D, F = (2, 1, 1024, 3584)
        return {'x': _w((lambda *s: torch.randn(*s, dtype=torch.bfloat16) * 0.05)(B, S, D)), 'w1': _w((lambda *s: torch.randn(*s, dtype=torch.bfloat16) * 3.2)(D, F) * D ** (-0.5)), 'b1': _w((lambda *s: torch.randn(*s, dtype=torch.bfloat16) * 3.2)(F)), 'w2': _w((lambda *s: torch.randn(*s, dtype=torch.bfloat16) * 0.2)(F, D) * F ** (-0.5)), 'b2': _w((lambda *s: torch.randn(*s, dtype=torch.bfloat16) * 0.4)(D))}
    _B_in = _B_setup()
    return {'A_x': _A_in['x'], 'A_k_cache': _A_in['k_cache'], 'A_v_cache': _A_in['v_cache'], 'A_wq': _A_in['wq'], 'A_wo': _A_in['wo'], 'B_x': _B_in['x'], 'B_w1': _B_in['w1'], 'B_b1': _B_in['b1'], 'B_w2': _B_in['w2'], 'B_b2': _B_in['b2'], 'c': _w((lambda *s: torch.randn(*s, dtype=torch.bfloat16) * 0.2)(2, 1024) * 8.0), 'w_mod': _w((lambda *s: torch.randn(*s, dtype=torch.bfloat16) * 0.2)(1024, 6 * 1024) * 1024 ** (-0.5) * 8.0)}

def reference(A_x, A_k_cache, A_v_cache, A_wq, A_wo, B_x, B_w1, B_b1, B_w2, B_b2, c, w_mod):

    def _A_ref(x, k_cache, v_cache, wq, wo):
        B, Sq, D = x.shape
        Hq, Dh = (32, 128)
        Hkv, Sk = (k_cache.shape[1], k_cache.shape[2])
        W = min(4096, max(256, Sk // 4))
        rep = Hq // Hkv
        q = (lambda x, *d: x.permute(*d))((x @ wq).reshape(B, Sq, Hq, Dh), 0, 2, 1, 3)
        k = (lambda x, n, axis: torch.repeat_interleave(x, n, dim=axis))(k_cache[:, :, Sk - W:, :], rep, axis=1)
        v = (lambda x, n, axis: torch.repeat_interleave(x, n, dim=axis))(v_cache[:, :, Sk - W:, :], rep, axis=1)
        scores = torch.einsum('bhqd,bhkd->bhqk', q, k) * Dh ** (-0.5)
        p = _softmax(scores, axis=-1)
        o = torch.einsum('bhqk,bhkd->bhqd', p, v)
        return (lambda x, *d: x.permute(*d))(o, 0, 2, 1, 3).reshape(B, Sq, Hq * Dh) @ wo

    def _B_ref(x, w1, b1, w2, b2):
        y = _layernorm(x, axis=-1)
        h = _gelu(torch.einsum('bsd,df->bsf', y, w1) + b1)
        return torch.einsum('bsf,fd->bsd', h, w2) + b2
    x = A_x
    mod = (_silu(c) @ w_mod).reshape(c.shape[0], 6, 1, -1)
    sa, ca, ga, sb, cb, gb = (mod[:, 0].clone(), mod[:, 1].clone(), mod[:, 2].clone(), mod[:, 3].clone(), mod[:, 4].clone(), mod[:, 5].clone())
    h = _layernorm(x, axis=-1) * (1 + ca) + sa
    x = x + ga * _A_ref(h, A_k_cache, A_v_cache, A_wq, A_wo)
    h = _layernorm(x, axis=-1) * (1 + cb) + sb
    x = x + gb * _B_ref(h, B_w1, B_b1, B_w2, B_b2)
    return x
```

```cpp
#include <hip/hip_runtime.h>
#include <hip/hip_bf16.h>
#include <stdint.h>

#define EMU_SCORE 0
#define EMU_ALL 1

typedef unsigned short u16;
__device__ __forceinline__ float bf2f(u16 b) { return __builtin_bit_cast(float, (unsigned)b << 16); }
__device__ __forceinline__ u16 f2bf(float f) { unsigned u = __builtin_bit_cast(unsigned, f); u += 0x7FFFu + ((u >> 16) & 1u); return (u16)(u >> 16); }
__device__ __forceinline__ float rb(float f) {
  return bf2f(f2bf(f));
}
__device__ __forceinline__ float rbs(float f) {
  return f;
}

constexpr int D = 1024, HQ = 32, HKV = 4, SK = 32768, DH = 128, W = 4096, F = 3584;

__device__ float block_sum(float v, float* red) {
  for (int o = 32; o > 0; o >>= 1) v += __shfl_xor(v, o);
  int w = threadIdx.x >> 6, nw = blockDim.x >> 6;
  __syncthreads();
  if ((threadIdx.x & 63) == 0) red[w] = v;
  __syncthreads();
  float s = 0;
  for (int i = 0; i < nw; ++i) s += red[i];
  return s;
}

__global__ void gemv_k(const float* in, const u16* w, float* out, int K, int N) {
  __shared__ float red[2][4][64];
  int n = blockIdx.x * 64 + (threadIdx.x & 63), kg = threadIdx.x >> 6;
  float a0 = 0, a1 = 0;
  for (int k = kg; k < K; k += 4) { float wv = bf2f(w[(size_t)k * N + n]); a0 += in[k] * wv; a1 += in[K + k] * wv; }
  red[0][kg][threadIdx.x & 63] = a0; red[1][kg][threadIdx.x & 63] = a1;
  __syncthreads();
  if (kg < 2) { int c = threadIdx.x & 63; float s = red[kg][0][c] + red[kg][1][c] + red[kg][2][c] + red[kg][3][c]; out[kg * N + n] = rb(s); }
}

__global__ void silu_k(const u16* c, float* out) { int i = blockIdx.x * 256 + threadIdx.x; float v = bf2f(c[i]); out[i] = rb(v / (1.f + __expf(-v))); }

__device__ float ln_elem(float x, float* red) {
  float s = block_sum(x, red); float mean = s * (1.f / D);
  float d = x - mean; float v = block_sum(d * d, red) * (1.f / D);
  float mb = rb(mean), vb = rb(v);
  float num = rb(x - mb); float den = rb(sqrtf(rb(vb + 1e-5f)));
  return rb(num / den);
}

__global__ void h1_k(const u16* x, const float* mod, float* h) {
  __shared__ float red[16];
  int b = blockIdx.x, i = threadIdx.x;
  float xv = bf2f(x[b * D + i]);
  float y = ln_elem(xv, red);
  float sa = mod[b * 6144 + i], ca = mod[b * 6144 + 1024 + i];
  h[b * D + i] = rb(rb(y * rb(1.f + ca)) + sa);
}

__global__ void attn_k(const float* q, const u16* kc, const u16* vc, float* o) {
  __shared__ float sc[W]; __shared__ float red[16]; __shared__ float qs[DH]; __shared__ float ored[2][DH];
  int b = blockIdx.x / HQ, h = blockIdx.x % HQ, hk = h / (HQ / HKV);
  const u16* kb = kc + ((size_t)(b * HKV + hk) * SK + (SK - W)) * DH;
  const u16* vb = vc + ((size_t)(b * HKV + hk) * SK + (SK - W)) * DH;
  if (threadIdx.x < DH) qs[threadIdx.x] = q[b * 4096 + h * DH + threadIdx.x];
  __syncthreads();
  float mx = -INFINITY;
  for (int j = threadIdx.x; j < W; j += 256) {
    float s = 0; for (int d = 0; d < DH; ++d) s += qs[d] * bf2f(kb[(size_t)j * DH + d]);
    s = rbs(rbs(s) * 0.08838834764831845f);
    sc[j] = s; mx = fmaxf(mx, s);
  }
  for (int o2 = 32; o2 > 0; o2 >>= 1) mx = fmaxf(mx, __shfl_xor(mx, o2));
  __syncthreads();
  if ((threadIdx.x & 63) == 0) red[threadIdx.x >> 6] = mx;
  __syncthreads();
  mx = fmaxf(fmaxf(red[0], red[1]), fmaxf(red[2], red[3]));
  float sum = 0;
  for (int j = threadIdx.x; j < W; j += 256) { float e = __expf(sc[j] - mx); sc[j] = e; sum += e; }
  sum = block_sum(sum, red);
  __syncthreads();
  int d = threadIdx.x & 127, g = threadIdx.x >> 7;
  float acc = 0;
  for (int j = g; j < W; j += 2) acc += rb(sc[j] / sum) * bf2f(vb[(size_t)j * DH + d]);
  ored[g][d] = acc;
  __syncthreads();
  if (threadIdx.x < DH) o[b * 4096 + h * DH + threadIdx.x] = rb(ored[0][threadIdx.x] + ored[1][threadIdx.x]);
}

__global__ void mid_k(const u16* x, const float* mod, const float* attn, float* x2, float* y) {
  __shared__ float red[16];
  int b = blockIdx.x, i = threadIdx.x;
  float ga = mod[b * 6144 + 2048 + i], sb = mod[b * 6144 + 3072 + i], cb = mod[b * 6144 + 4096 + i];
  float xv = rb(bf2f(x[b * D + i]) + rb(ga * attn[b * D + i]));
  x2[b * D + i] = xv;
  float l = ln_elem(xv, red);
  float h = rb(rb(l * rb(1.f + cb)) + sb);
  y[b * D + i] = ln_elem(h, red);
}

__global__ void gelu_k(const u16* b1, float* z) {
  int i = blockIdx.x * 256 + threadIdx.x; int n = i % F;
  float v = rb(z[i] + bf2f(b1[n]));
  float t = tanhf(0.7978845608028654f * (v + 0.044715f * v * v * v));
  z[i] = rb(0.5f * v * (1.f + t));
}

__global__ void fin_k(const u16* b2, const float* mod, const float* x2, const float* m, u16* out) {
  int i = blockIdx.x * 256 + threadIdx.x; int b = i / D, n = i % D;
  float gb = mod[b * 6144 + 5120 + n];
  float v = rb(m[i] + bf2f(b2[n]));
  out[i] = f2bf(x2[i] + rb(gb * v));
}

extern "C" void kernel_launch(void* const* d_in, const int* in_sizes, int n_in, void* d_out, int out_size, void* d_ws, size_t ws_size, hipStream_t stream) {
  const u16* x = (const u16*)d_in[0]; const u16* kc = (const u16*)d_in[1]; const u16* vc = (const u16*)d_in[2];
  const u16* wq = (const u16*)d_in[3]; const u16* wo = (const u16*)d_in[4];
  const u16* w1 = (const u16*)d_in[6]; const u16* b1 = (const u16*)d_in[7]; const u16* w2 = (const u16*)d_in[8]; const u16* b2 = (const u16*)d_in[9];
  const u16* c = (const u16*)d_in[10]; const u16* wmod = (const u16*)d_in[11];
  float* ws = (float*)d_ws;
  float* sc = ws; float* mod = ws + 2048; float* h = mod + 12288; float* q = h + 2048; float* o = q + 8192; float* attn = o + 8192;
  float* x2 = attn + 2048; float* y = x2 + 2048; float* z = y + 2048; float* m = z + 7168;
  silu_k<<<8, 256, 0, stream>>>(c, sc);
  gemv_k<<<96, 256, 0, stream>>>(sc, wmod, mod, 1024, 6144);
  h1_k<<<2, 1024, 0, stream>>>(x, mod, h);
  gemv_k<<<64, 256, 0, stream>>>(h, wq, q, 1024, 4096);
  attn_k<<<64, 256, 0, stream>>>(q, kc, vc, o);
  gemv_k<<<16, 256, 0, stream>>>(o, wo, attn, 4096, 1024);
  mid_k<<<2, 1024, 0, stream>>>(x, mod, attn, x2, y);
  gemv_k<<<56, 256, 0, stream>>>(y, w1, z, 1024, 3584);
  gelu_k<<<28, 256, 0, stream>>>(b1, z);
  gemv_k<<<16, 256, 0, stream>>>(z, w2, m, 3584, 1024);
  fin_k<<<8, 256, 0, stream>>>(b2, mod, x2, m, (u16*)d_out);
}
```

```cpp
#include <hip/hip_runtime.h>
#include <hip/hip_bf16.h>
#include <stdint.h>

namespace {

typedef unsigned short u16;
typedef unsigned long long u64;
typedef unsigned v4u __attribute__((ext_vector_type(4)));
typedef unsigned v2u __attribute__((ext_vector_type(2)));
typedef float f32x4 __attribute__((ext_vector_type(4)));
typedef float f32x2 __attribute__((ext_vector_type(2)));
typedef __bf16 bf16x8 __attribute__((ext_vector_type(8)));
typedef __bf16 bf16x2 __attribute__((ext_vector_type(2)));
typedef short s16x4 __attribute__((ext_vector_type(4)));
#define GAS __attribute__((address_space(1)))
#define LAS __attribute__((address_space(3)))

constexpr int NTHR = 576;
constexpr unsigned SENT = 0xFFFFFFFFu;
constexpr u64 TAGK = 0x9E3779B97F4A7C15ull;
constexpr int SPIN_CAP = 1 << 16;

constexpr int WS_EPOCH = 0;
constexpr int WS_FLAGS = 4096;
constexpr int WS_E1 = 8192;
constexpr int WS_E2 = WS_E1 + 65536;
constexpr int WS_E3O = WS_E2 + 65536;
constexpr int WS_E3S = WS_E3O + 524288;
constexpr int WS_E4 = WS_E3S + 8192;
constexpr int WS_E5 = WS_E4 + 1048576;
constexpr int WS_EL = WS_E5 + 8192;
constexpr int WS_E6 = WS_EL + 16384;
constexpr int WS_END = WS_E6 + 458752;

constexpr int L_K = 0, L_V = 65536;
constexpr int L_ADUP = 131072;
constexpr int L_RED = L_ADUP + 8704;
constexpr int L_QS = L_RED + 8192;
constexpr int L_ML = L_QS + 2304;
constexpr int L_ADR = L_ML + 512;
constexpr int L_LN = L_ADR + 640;
constexpr int L_END = L_LN + 512;
constexpr int LDS_BYTES = L_END;

#define D_K 130
#define D_LA 390
#define D_LB 450
#define D_LC 600
#define D_M 450

struct Params {
  const u16 *x, *kc, *vc, *wq, *wo, *w1, *b1, *w2, *b2, *c, *wmod;
  u16* out;
  unsigned char* ws;
};

__device__ __forceinline__ u64 rt() { return __builtin_amdgcn_s_memrealtime(); }
__device__ __forceinline__ void wait_until(u64 t0, int d) { while ((long long)(rt() - t0) < d) {} }
__device__ __forceinline__ float u2f(unsigned u) { return __builtin_bit_cast(float, u); }
__device__ __forceinline__ unsigned f2u(float f) { return __builtin_bit_cast(unsigned, f); }
__device__ __forceinline__ float bf2f(unsigned b) { return __builtin_bit_cast(float, b << 16); }
__device__ __forceinline__ float bfhi(unsigned b) { return __builtin_bit_cast(float, b & 0xFFFF0000u); }
__device__ __forceinline__ unsigned pk2(float lo, float hi) { f32x2 v = {lo, hi}; return __builtin_bit_cast(unsigned, __builtin_convertvector(v, bf16x2)); }
__device__ __forceinline__ float rb(float f) { return bfhi(pk2(f, f)); }
__device__ __forceinline__ unsigned san(float f) { return min(__builtin_bit_cast(unsigned, f), 0xFFFFFFFEu); }
__device__ __forceinline__ unsigned sanu(unsigned u) { return min(u, 0xFFFFFFFEu); }
__device__ __forceinline__ f32x4 mfma(v4u a, v4u b, f32x4 c) {
  return __builtin_amdgcn_mfma_f32_16x16x32_bf16(__builtin_bit_cast(bf16x8, a), __builtin_bit_cast(bf16x8, b), c, 0, 0, 0);
}
#define BAR() asm volatile("s_waitcnt lgkmcnt(0)\n\ts_barrier" ::: "memory")
#define VMCNT(n) asm volatile("s_waitcnt vmcnt(" #n ")" ::: "memory")
#define SB() __builtin_amdgcn_sched_barrier(0)
constexpr int SC1 = 16;
typedef __amdgpu_buffer_rsrc_t rsrc_t;
__device__ __forceinline__ v4u ld16(rsrc_t rs, int off) { return __builtin_amdgcn_raw_buffer_load_b128(rs, off, 0, SC1); }
__device__ __forceinline__ v2u ld8(rsrc_t rs, int off) { return __builtin_amdgcn_raw_buffer_load_b64(rs, off, 0, SC1); }
__device__ __forceinline__ void st16(rsrc_t rs, int off, v4u v) { __builtin_amdgcn_raw_buffer_store_b128(v, rs, off, 0, SC1); }
__device__ __forceinline__ void st8(rsrc_t rs, int off, v2u v) { __builtin_amdgcn_raw_buffer_store_b64(v, rs, off, 0, SC1); }
__device__ __forceinline__ v4u ldnt(const void* p) { return __builtin_nontemporal_load((const GAS v4u*)p); }
__device__ __forceinline__ bool ok4(v4u v) { return (v.x != SENT) & (v.y != SENT) & (v.z != SENT) & (v.w != SENT); }
__device__ __forceinline__ bool ok2(v2u v) { return (v.x != SENT) & (v.y != SENT); }
__device__ __forceinline__ f32x4 asf(v4u v) { return __builtin_bit_cast(f32x4, v); }
__device__ __forceinline__ v4u san4(f32x4 v) { return v4u{san(v.x), san(v.y), san(v.z), san(v.w)}; }

template <int CTRL> __device__ __forceinline__ float dpp(float x) {
  return __builtin_bit_cast(float, __builtin_amdgcn_mov_dpp(__builtin_bit_cast(int, x), CTRL, 0xf, 0xf, true));
}
constexpr int XOR1 = 0xB1, XOR2 = 0x4E, ROR8 = 0x128, ROR4 = 0x124;
__device__ __forceinline__ float x16(float x) {
  auto s = __builtin_amdgcn_permlane16_swap(__builtin_bit_cast(unsigned, x), __builtin_bit_cast(unsigned, x), false, false);
  return u2f(s[0] ^ s[1] ^ f2u(x));
}
__device__ __forceinline__ float x32(float x) {
  auto s = __builtin_amdgcn_permlane32_swap(__builtin_bit_cast(unsigned, x), __builtin_bit_cast(unsigned, x), false, false);
  return u2f(s[0] ^ s[1] ^ f2u(x));
}
__device__ __forceinline__ float sum16(float x) { auto s = __builtin_amdgcn_permlane16_swap(__builtin_bit_cast(unsigned, x), __builtin_bit_cast(unsigned, x), false, false); return u2f(s[0]) + u2f(s[1]); }
__device__ __forceinline__ float sum32(float x) { auto s = __builtin_amdgcn_permlane32_swap(__builtin_bit_cast(unsigned, x), __builtin_bit_cast(unsigned, x), false, false); return u2f(s[0]) + u2f(s[1]); }
__device__ __forceinline__ float max16(float x) { auto s = __builtin_amdgcn_permlane16_swap(__builtin_bit_cast(unsigned, x), __builtin_bit_cast(unsigned, x), false, false); return fmaxf(u2f(s[0]), u2f(s[1])); }
__device__ __forceinline__ float max32(float x) { auto s = __builtin_amdgcn_permlane32_swap(__builtin_bit_cast(unsigned, x), __builtin_bit_cast(unsigned, x), false, false); return fmaxf(u2f(s[0]), u2f(s[1])); }

#define STAMP(i) do {} while (0)

__device__ __forceinline__ void dma16(const void* gsrc, unsigned ldsaddr) {
  unsigned keep;
  asm volatile("s_mov_b32 %0, m0\n\ts_mov_b32 m0, %2\n\ts_nop 0\n\tglobal_load_lds_dwordx4 %1, off nt\n\ts_mov_b32 m0, %0"
               : "=&s"(keep) : "v"(gsrc), "s"(ldsaddr) : "memory");
}

template <int NL>
__device__ __forceinline__ void load_col(v4u (&r)[NL], const u16* base, int row0, int stride_b, int lane) {
  const char* src = (const char*)base + (size_t)(row0 + (lane >> 3)) * stride_b + (lane & 7) * 16;
#pragma unroll
  for (int i = 0; i < NL; ++i) r[i] = ldnt(src + (size_t)i * 8 * stride_b);
  SB();
}
struct Masks { unsigned m0, m1, m2, m3; };
__device__ __forceinline__ v4u mkA(unsigned xx, const Masks& k) { return v4u{xx & k.m0, xx & k.m1, xx & k.m2, xx & k.m3}; }

template <int NL, int STR>
__device__ __forceinline__ void col_mfma(const v4u (&wt)[NL], const LAS unsigned* adup, int col0, const Masks& mk, int lane, f32x4& d0, f32x4& d1) {
  const int r8 = 2 * (lane >> 4) + ((lane >> 3) & 1);
  const LAS v4u* p0 = (const LAS v4u*)(adup + r8 * STR + col0);
  const LAS v4u* p1 = (const LAS v4u*)(adup + (8 + r8) * STR + col0);
#pragma unroll
  for (int t4 = 0; t4 < NL / 4; ++t4) {
    const v4u x0 = p0[t4], x1 = p1[t4];
#pragma unroll
    for (int u = 0; u < 4; ++u) {
      d0 = mfma(mkA(x0[u], mk), wt[t4 * 4 + u], d0);
      d1 = mfma(mkA(x1[u], mk), wt[t4 * 4 + u], d1);
    }
  }
}
__device__ __forceinline__ void col_red_store(LAS float* red, int w, int lane, f32x4 d0, f32x4 d1) {
  const int half = lane >> 5;
  if (half == ((lane >> 3) & 1)) {
    LAS float* p = red + ((w * 2 + half) * 2) * 64 + 8 * (lane & 7) + 4 * ((lane >> 4) & 1);
    *(LAS f32x4*)p = d0;
    *(LAS f32x4*)(p + 64) = d1;
  }
}
__device__ __forceinline__ f32x4 col_red_sum(const LAS float* red, int lane) {
  const LAS f32x4* p = (const LAS f32x4*)(red + (lane >> 5) * 8 * 128 + (lane & 31) * 4);
  f32x4 s = p[0];
#pragma unroll
  for (int i = 1; i < 8; ++i) s += p[i * 32];
  s.x = sum32(s.x); s.y = sum32(s.y); s.z = sum32(s.z); s.w = sum32(s.w);
  return s;
}
template <int NL, int STR>
__device__ __forceinline__ f32x4 row_mfma(const v4u (&wt)[NL], const LAS unsigned* adr, const Masks& mk, int lane) {
  const LAS v4u* p = (const LAS v4u*)(adr + (((lane >> 3) & 1) * 4 + (lane >> 4)) * STR);
  f32x4 d = {0.f, 0.f, 0.f, 0.f};
#pragma unroll
  for (int t4 = 0; t4 < NL / 4; ++t4) {
    const v4u x = p[t4];
#pragma unroll
    for (int u = 0; u < 4; ++u) d = mfma(mkA(x[u], mk), wt[t4 * 4 + u], d);
  }
  return d;
}

__device__ __forceinline__ float silu_rb(float v) { return rb(v / (1.f + __expf(-v))); }
__device__ __forceinline__ float ln_apply(float x, float mb, float den) { return rb(rb(x - mb) / den); }
__device__ __forceinline__ float ln_den(float var) { return rb(sqrtf(rb(rb(var) + 1e-5f))); }

__device__ __forceinline__ void chan(float& m, float& q, float m2, float q2, float halfn) {
  const float d = m2 - m;
  m = 0.5f * (m + m2);
  q = q + q2 + d * d * halfn;
}

__device__ __forceinline__ void clear_range(rsrc_t rs, int off, int bytes, int lane) {
  const v4u s = {SENT, SENT, SENT, SENT};
  for (int o = lane * 16; o < bytes; o += 1024) st16(rs, off + o, s);
}

__global__ void __launch_bounds__(NTHR) fused_kernel(Params p) {
  extern __shared__ __attribute__((aligned(16))) char smem[];
  LAS char* L = (LAS char*)smem;
  const u64 t0 = rt();
  const int tid = threadIdx.x, lane = tid & 63, w = __builtin_amdgcn_readfirstlane(tid >> 6), bid = blockIdx.x;
  const rsrc_t rs = __builtin_amdgcn_make_buffer_rsrc((void*)p.ws, 0,
                                                      WS_END,
                                                      0x00020000);
  Masks mk;
  {
    const int e = lane & 7, d = e >> 1;
    const unsigned hm = (e & 1) ? 0xFFFF0000u : 0x0000FFFFu;
    mk.m0 = d == 0 ? hm : 0u; mk.m1 = d == 1 ? hm : 0u; mk.m2 = d == 2 ? hm : 0u; mk.m3 = d == 3 ? hm : 0u;
  }
  LAS unsigned* adup = (LAS unsigned*)(L + L_ADUP);
  LAS float* red = (LAS float*)(L + L_RED);
  LAS unsigned* adr = (LAS unsigned*)(L + L_ADR);
  const bool svc = (w == 8);
  u64 tag = 0;
  if (w == 0) STAMP(0);

  if (svc) {
    const v2u e = ld8(rs, WS_EPOCH);
    if (bid < 128) {
      const int j = bid, kq = j & 3, cg = j >> 2;
      if (lane < 32) st16(rs, WS_E1 + ((kq * 2 + (lane >> 4)) * 2048 + cg * 64) * 4 + (lane & 15) * 16, v4u{SENT, SENT, SENT, SENT});
      const int b = j >> 6, hkv = (j >> 4) & 3, s = j & 15;
#pragma unroll
      for (int i = 0; i < 4; ++i) {
        const int h = i * 2 + (lane >> 5);
        st16(rs, WS_E3O + (((b * 32 + hkv * 8 + h) * 16 + s) * 128) * 4 + (lane & 31) * 16, v4u{SENT, SENT, SENT, SENT});
      }
      if (lane < 8) st8(rs, WS_E3S + ((b * 32 + hkv * 8 + lane) * 16 + s) * 8, v2u{SENT, SENT});
      clear_range(rs, WS_E4 + j * 8192, 8192, lane);
      if (j < 64 && lane < 8) st16(rs, WS_E5 + j * 128 + lane * 16, v4u{SENT, SENT, SENT, SENT});
    } else {
      const int j = bid - 128, kh = j & 1, cg = j >> 1;
      if (lane < 32) st16(rs, WS_E2 + ((kh * 2 + (lane >> 4)) * 4096 + cg * 64) * 4 + (lane & 15) * 16, v4u{SENT, SENT, SENT, SENT});
      if (j < 64) { if (lane < 16) st16(rs, WS_EL + ((lane >> 3) * 4096 + j * 64) * 2 + (lane & 7) * 16, v4u{SENT, SENT, SENT, SENT}); }
      else if (j < 120) clear_range(rs, WS_E6 + (j - 64) * 8192, 8192, lane);
    }
    VMCNT(0);
    tag = (((u64)e.y << 32) | e.x) + TAGK;
    if (lane == 0) st8(rs, WS_FLAGS + bid * 8, v2u{(unsigned)tag, (unsigned)(tag >> 32)});
  }
  auto check_flags = [&]() {
    for (int spin = 0; spin < SPIN_CAP; ++spin) {
      asm volatile("" ::: "memory");
      const v4u a = ld16(rs, WS_FLAGS + lane * 32), b = ld16(rs, WS_FLAGS + lane * 32 + 16);
      const unsigned lo = (unsigned)tag, hi = (unsigned)(tag >> 32);
      const bool ok = (a.x == lo) & (a.y == hi) & (a.z == lo) & (a.w == hi) & (b.x == lo) & (b.y == hi) & (b.z == lo) & (b.w == hi);
      if (__all(ok)) break;
    }
  };

  if (bid < 128) {
    const int j = bid;
    v4u wo_t[8];
    if (!svc) {
      const int kq = j & 3, cg = j >> 2;
      v4u wt[4];
      const unsigned craw = p.c[(tid >> 8) * 1024 + kq * 256 + (tid & 255)];
      SB();
      load_col<4>(wt, p.wmod + cg * 64, kq * 256 + w * 32, 12288, lane);
      {
        const int b = tid >> 8, k = tid & 255;
        const float s = silu_rb(bf2f(craw));
        adup[(b * 8 + (k & 7)) * 36 + (k >> 3)] = pk2(s, s);
      }
      BAR();
      f32x4 d0 = {0.f, 0.f, 0.f, 0.f}, d1 = d0;
      col_mfma<4, 36>(wt, adup, w * 4, mk, lane, d0, d1);
      col_red_store(red, w, lane, d0, d1);
      if (w == 0) STAMP(1);
      BAR();
      if (w == 0) {
        const f32x4 s = col_red_sum(red, lane);
        if (lane < 32) st16(rs, WS_E1 + ((kq * 2 + (lane >> 4)) * 2048 + cg * 64) * 4 + (lane & 15) * 16, san4(s));
        STAMP(2);
      }
      const int b = j >> 6, hkv = (j >> 4) & 3, sp = j & 15;
      const size_t kvoff = ((size_t)(b * 4 + hkv) * 32768 + (32768 - 4096) + sp * 256 + w * 32) * 256;
      wait_until(t0, D_K);
      const unsigned lbase = (unsigned)(size_t)L;
#pragma unroll
      for (int i = 0; i < 8; ++i) {
        const int row = i * 4 + (lane >> 4), ch = (lane & 15) ^ (row & 15);
        dma16((const char*)p.kc + kvoff + row * 256 + ch * 16, lbase + L_K + w * 8192 + i * 1024);
      }
#pragma unroll
      for (int i = 0; i < 8; ++i) {
        const int row = i * 4 + (lane >> 4), ch = (lane & 15) ^ ((row & 7) << 1);
        dma16((const char*)p.vc + kvoff + row * 256 + ch * 16, lbase + L_V + w * 8192 + i * 1024);
      }
      {
        const char* src = (const char*)p.wo + (size_t)(j * 32 + (lane >> 4)) * 2048 + w * 256 + (lane & 15) * 16;
#pragma unroll
        for (int i = 0; i < 8; ++i) wo_t[i] = ldnt(src + (size_t)i * 4 * 2048);
        SB();
      }
      BAR();
      if (w == 0) STAMP(4);
      VMCNT(16);
      if (w == 0) STAMP(5);
      const int g = lane >> 4, c = lane & 15;
      f32x4 sc[2];
      {
        v4u qf[4];
#pragma unroll
        for (int s = 0; s < 4; ++s) qf[s] = *(const LAS v4u*)(L + L_QS + (c & 7) * 272 + (s * 4 + g) * 16);
#pragma unroll
        for (int T = 0; T < 2; ++T) {
          f32x4 d = {0.f, 0.f, 0.f, 0.f};
          const int row = T * 16 + c;
#pragma unroll
          for (int s = 0; s < 4; ++s) {
            const v4u kf = *(const LAS v4u*)(L + L_K + w * 8192 + row * 256 + (((s * 4 + g) ^ (row & 15)) * 16));
            d = mfma(kf, qf[s], d);
          }
          sc[T] = d;
        }
      }
      float mx = -INFINITY;
#pragma unroll
      for (int T = 0; T < 2; ++T)
#pragma unroll
        for (int r = 0; r < 4; ++r) { const float s = rb(rb(sc[T][r]) * 0.08838834764831845f); sc[T][r] = s; mx = fmaxf(mx, s); }
      mx = max32(max16(mx));
      float ls = 0.f;
#pragma unroll
      for (int T = 0; T < 2; ++T)
#pragma unroll
        for (int r = 0; r < 4; ++r) { const float e = __expf(sc[T][r] - mx); sc[T][r] = e; ls += e; }
      ls = sum32(sum16(ls));
      const v4u pa = {pk2(sc[0][0], sc[0][1]), pk2(sc[0][2], sc[0][3]), pk2(sc[1][0], sc[1][1]), pk2(sc[1][2], sc[1][3])};
      {
        v4u pin = pa; asm volatile("" : "+v"(pin)); (void)pin;
      }
      SB();
      VMCNT(8);
      if (w == 0) STAMP(6);
      LAS float* ow = (LAS float*)(L + L_K + w * 8192);
      {
        const int q = c >> 2, pp = c & 3;
        const int r0 = 4 * g + q, r1 = 16 + 4 * g + q;
        const LAS char* vb = L + L_V + w * 8192;
        f32x4 o[8];
#pragma unroll
        for (int dt = 0; dt < 8; ++dt) {
          const s16x4 b0 = __builtin_amdgcn_ds_read_tr16_b64_v4i16((LAS s16x4*)(vb + r0 * 256 + ((dt ^ (r0 & 7)) * 32) + pp * 8));
          const s16x4 b1 = __builtin_amdgcn_ds_read_tr16_b64_v4i16((LAS s16x4*)(vb + r1 * 256 + ((dt ^ (r1 & 7)) * 32) + pp * 8));
          const v2u u0 = __builtin_bit_cast(v2u, b0), u1 = __builtin_bit_cast(v2u, b1);
          o[dt] = mfma(pa, v4u{u0.x, u0.y, u1.x, u1.y}, f32x4{0.f, 0.f, 0.f, 0.f});
        }
        if (g < 2) {
#pragma unroll
          for (int dt = 0; dt < 8; ++dt)
#pragma unroll
            for (int r = 0; r < 4; ++r) ow[(4 * g + r) * 128 + dt * 16 + c] = o[dt][r];
        }
      }
      if (lane < 8) { LAS float* ml = (LAS float*)(L + L_ML) + (w * 8 + lane) * 2; ml[0] = mx; ml[1] = ls; }
      BAR();
      {
        const int h = w;
        const LAS float* ml = (const LAS float*)(L + L_ML) + h * 2;
        float mw[8], M = -INFINITY;
#pragma unroll
        for (int ww = 0; ww < 8; ++ww) { mw[ww] = ml[ww * 16]; M = fmaxf(M, mw[ww]); }
        float l = 0.f; f32x2 o = {0.f, 0.f};
#pragma unroll
        for (int ww = 0; ww < 8; ++ww) {
          const float f = __expf(mw[ww] - M);
          l += f * ml[ww * 16 + 1];
          const f32x2 v = *(const LAS f32x2*)(L + L_K + ww * 8192 + (h * 128 + lane * 2) * 4);
          o += f * v;
        }
        const int row = (b * 32 + hkv * 8 + h) * 16 + sp;
        st8(rs, WS_E3O + row * 512 + lane * 8, v2u{san(o.x), san(o.y)});
        if (lane == 0) st8(rs, WS_E3S + row * 8, v2u{san(M), san(l)});
        if (w == 0) STAMP(7);
      }
      BAR();
      if (w == 0) STAMP(8);
      {
        const f32x4 d = row_mfma<8, 12>(wo_t, adr, mk, lane);
        const int i = 8 * w + (c >> 1);
        st16(rs, WS_E4 + (j * 64 + i) * 128 + (g >> 1) * 64 + ((c & 1) * 8 + (g & 1) * 4) * 4, san4(d));
        if (w == 0) STAMP(9);
      }
    } else {
      BAR();
      BAR();
      check_flags();
      STAMP(3);
      const int b = j >> 6, hkv = (j >> 4) & 3;
      {
        v4u a[4], c2[4];
        for (int spin = 0; spin < SPIN_CAP; ++spin) {
          asm volatile("" ::: "memory");
          bool ok = true;
#pragma unroll
          for (int i = 0; i < 4; ++i) {
            const int v = hkv * 1024 + (i * 64 + lane) * 4;
            a[i] = ld16(rs, WS_E2 + ((0 * 2 + b) * 4096 + v) * 4);
            c2[i] = ld16(rs, WS_E2 + ((1 * 2 + b) * 4096 + v) * 4);
          }
#pragma unroll
          for (int i = 0; i < 4; ++i) ok &= ok4(a[i]) & ok4(c2[i]);
          if (__all(ok)) break;
        }
#pragma unroll
        for (int i = 0; i < 4; ++i) {
          const f32x4 s = asf(a[i]) + asf(c2[i]);
          const int v = (i * 64 + lane) * 4, h = v >> 7, d = v & 127;
          *(LAS v2u*)(L + L_QS + h * 272 + d * 2) = v2u{pk2(s.x, s.y), pk2(s.z, s.w)};
        }
      }
      BAR();
      BAR();
      {
        const int h = j >> 2, dq = j & 3, p8 = lane >> 3, ch = lane & 7;
        v4u o[4]; v2u st[4];
        for (int spin = 0; spin < SPIN_CAP; ++spin) {
          asm volatile("" ::: "memory");
          bool ok = true;
#pragma unroll
          for (int i = 0; i < 4; ++i) {
            const int row = ((i >> 1) * 32 + h) * 16 + (i & 1) * 8 + p8;
            o[i] = ld16(rs, WS_E3O + row * 512 + dq * 128 + ch * 16);
            st[i] = ld8(rs, WS_E3S + row * 8);
          }
#pragma unroll
          for (int i = 0; i < 4; ++i) ok &= ok4(o[i]) & ok2(st[i]);
          if (__all(ok)) break;
        }
#pragma unroll
        for (int bb = 0; bb < 2; ++bb) {
          const float m0 = u2f(st[bb * 2].x), m1 = u2f(st[bb * 2 + 1].x);
          float M = fmaxf(m0, m1);
          M = fmaxf(M, dpp<ROR8>(M)); M = max32(max16(M));
          const float f0 = __expf(m0 - M), f1 = __expf(m1 - M);
          float l = f0 * u2f(st[bb * 2].y) + f1 * u2f(st[bb * 2 + 1].y);
          l += dpp<ROR8>(l); l = sum32(sum16(l));
          f32x4 a = f0 * asf(o[bb * 2]) + f1 * asf(o[bb * 2 + 1]);
#pragma unroll
          for (int r = 0; r < 4; ++r) { float t = a[r]; t += dpp<ROR8>(t); t = sum32(sum16(t)); a[r] = rb(t / l); }
          if (lane < 8) {
#pragma unroll
            for (int r = 0; r < 4; ++r) adr[(bb * 4 + r) * 12 + ch] = pk2(a[r], a[r]);
          }
        }
      }
      BAR();
      if (j < 64) {
        const int p8 = lane >> 3, ch = lane & 7, bb = ch >> 2, col = j * 16 + (ch & 3) * 4;
        const v2u xr = *(const GAS v2u*)(p.x + bb * 1024 + col);
        const v2u b2r = *(const GAS v2u*)(p.b2 + col);
        v2u ga, gb;
        f32x4 at;
        {
          v4u a[16];
          for (int spin = 0; spin < SPIN_CAP; ++spin) {
            asm volatile("" ::: "memory");
            bool ok = true;
            ga = ld8(rs, WS_EL + (bb * 4096 + col) * 2);
#pragma unroll
            for (int i = 0; i < 16; ++i) a[i] = ld16(rs, WS_E4 + ((i * 8 + p8) * 64 + j) * 128 + ch * 16);
            ok &= ok2(ga);
#pragma unroll
            for (int i = 0; i < 16; ++i) ok &= ok4(a[i]);
            if (__all(ok)) break;
          }
          at = asf(a[0]);
#pragma unroll
          for (int i = 1; i < 16; ++i) at += asf(a[i]);
        }
        STAMP(10);
        float x2[4];
        const float xv[4] = {bf2f(xr.x & 0xFFFFu), bfhi(xr.x), bf2f(xr.y & 0xFFFFu), bfhi(xr.y)};
        const float gav[4] = {bf2f(ga.x & 0xFFFFu), bfhi(ga.x), bf2f(ga.y & 0xFFFFu), bfhi(ga.y)};
#pragma unroll
        for (int r = 0; r < 4; ++r) { float t = at[r]; t += dpp<ROR8>(t); t = sum32(sum16(t)); x2[r] = rb(xv[r] + rb(gav[r] * rb(t))); }
        if (lane < 8) st16(rs, WS_E5 + j * 128 + ch * 16, v4u{san(x2[0]), san(x2[1]), san(x2[2]), san(x2[3])});
        STAMP(11);
        f32x4 mo;
        {
          v4u a[7];
          for (int spin = 0; spin < SPIN_CAP; ++spin) {
            asm volatile("" ::: "memory");
            bool ok = true;
            gb = ld8(rs, WS_EL + (bb * 4096 + 3072 + col) * 2);
#pragma unroll
            for (int i = 0; i < 7; ++i) a[i] = ld16(rs, WS_E6 + ((i * 8 + p8) * 64 + j) * 128 + ch * 16);
            ok &= ok2(gb);
#pragma unroll
            for (int i = 0; i < 7; ++i) ok &= ok4(a[i]);
            if (__all(ok)) break;
          }
          mo = asf(a[0]);
#pragma unroll
          for (int i = 1; i < 7; ++i) mo += asf(a[i]);
        }
        const float b2v[4] = {bf2f(b2r.x & 0xFFFFu), bfhi(b2r.x), bf2f(b2r.y & 0xFFFFu), bfhi(b2r.y)};
        const float gbv[4] = {bf2f(gb.x & 0xFFFFu), bfhi(gb.x), bf2f(gb.y & 0xFFFFu), bfhi(gb.y)};
        float ov[4];
#pragma unroll
        for (int r = 0; r < 4; ++r) { float t = mo[r]; t += dpp<ROR8>(t); t = sum32(sum16(t)); ov[r] = x2[r] + rb(gbv[r] * rb(rb(t) + b2v[r])); }
        if (lane < 8) *(GAS v2u*)(p.out + bb * 1024 + col) = v2u{pk2(ov[0], ov[1]), pk2(ov[2], ov[3])};
        if (j == 0 && lane == 0) { const u64 e1 = tag - TAGK + 1; st8(rs, WS_EPOCH, v2u{(unsigned)e1, (unsigned)(e1 >> 32)}); }
        STAMP(12);
      }
    }
  } else {
    const int j = bid - 128, kh = j & 1, cg = j >> 1;
    if (!svc) {
      v4u wt[8];
      float xn[4] = {0.f, 0.f, 0.f, 0.f};
      const int eb = (tid >> 7) & 1, ek = kh * 512 + (tid & 127) * 4;
      const v4u a = *(const GAS v4u*)(p.x + eb * 1024 + lane * 16), a2 = *(const GAS v4u*)(p.x + eb * 1024 + lane * 16 + 8);
      const v2u own = *(const GAS v2u*)(p.x + eb * 1024 + ek);
      SB();
      load_col<8>(wt, p.wq + cg * 64, kh * 512 + w * 64, 8192, lane);
      if (w < 4) {
        float v[16];
#pragma unroll
        for (int i = 0; i < 4; ++i) { v[2 * i] = bf2f(a[i] & 0xFFFFu); v[2 * i + 1] = bfhi(a[i]); v[8 + 2 * i] = bf2f(a2[i] & 0xFFFFu); v[9 + 2 * i] = bfhi(a2[i]); }
        float s = 0.f;
#pragma unroll
        for (int i = 0; i < 16; ++i) s += v[i];
        s += dpp<XOR1>(s); s += dpp<XOR2>(s); s += dpp<ROR4>(s); s += dpp<ROR8>(s); s = sum32(sum16(s));
        const float mean = s * (1.f / 1024.f);
        float q = 0.f;
#pragma unroll
        for (int i = 0; i < 16; ++i) { const float d = v[i] - mean; q += d * d; }
        q += dpp<XOR1>(q); q += dpp<XOR2>(q); q += dpp<ROR4>(q); q += dpp<ROR8>(q); q = sum32(sum16(q));
        const float mb = rb(mean), den = ln_den(q * (1.f / 1024.f));
        xn[0] = ln_apply(bf2f(own.x & 0xFFFFu), mb, den); xn[1] = ln_apply(bfhi(own.x), mb, den);
        xn[2] = ln_apply(bf2f(own.y & 0xFFFFu), mb, den); xn[3] = ln_apply(bfhi(own.y), mb, den);
      }
      BAR();
      if (w == 0) STAMP(1);
      if (w < 4) {
        v4u sa[4], ca[4];
        for (int spin = 0; spin < SPIN_CAP; ++spin) {
          asm volatile("" ::: "memory");
          bool ok = true;
#pragma unroll
          for (int q = 0; q < 4; ++q) {
            sa[q] = ld16(rs, WS_E1 + ((q * 2 + eb) * 2048 + ek) * 4);
            ca[q] = ld16(rs, WS_E1 + ((q * 2 + eb) * 2048 + 1024 + ek) * 4);
          }
#pragma unroll
          for (int q = 0; q < 4; ++q) ok &= ok4(sa[q]) & ok4(ca[q]);
          if (__all(ok)) break;
        }
        const f32x4 s = (asf(sa[0]) + asf(sa[1])) + (asf(sa[2]) + asf(sa[3]));
        const f32x4 cc = (asf(ca[0]) + asf(ca[1])) + (asf(ca[2]) + asf(ca[3]));
#pragma unroll
        for (int r = 0; r < 4; ++r) {
          const float h = rb(rb(xn[r] * rb(1.f + rb(cc[r]))) + rb(s[r]));
          const int k = (tid & 127) * 4 + r;
          adup[(eb * 8 + (k & 7)) * 68 + (k >> 3)] = pk2(h, h);
        }
        if (w == 0) STAMP(2);
      }
      BAR();
      f32x4 d0 = {0.f, 0.f, 0.f, 0.f}, d1 = d0;
      col_mfma<8, 68>(wt, adup, w * 8, mk, lane, d0, d1);
      col_red_store(red, w, lane, d0, d1);
      BAR();
      if (w == 0) {
        const f32x4 s = col_red_sum(red, lane);
        if (lane < 32) st16(rs, WS_E2 + ((kh * 2 + (lane >> 4)) * 4096 + cg * 64) * 4 + (lane & 15) * 16, san4(s));
        STAMP(3);
      }
    } else {
      check_flags();
      BAR();
      BAR();
      BAR();
    }
    if (j < 64) {
      if (!svc) {
        wait_until(t0, j < 16 ? D_LA : (j < 48 ? D_LB : D_LC));
        v4u wt[16];
        const int b = tid >> 8, k0 = (tid & 255) * 4;
        const v2u cv = *(const GAS v2u*)(p.c + b * 1024 + k0);
        SB();
        load_col<16>(wt, p.wmod + 2048 + j * 64, w * 128, 12288, lane);
        const float c4[4] = {bf2f(cv.x & 0xFFFFu), bfhi(cv.x), bf2f(cv.y & 0xFFFFu), bfhi(cv.y)};
#pragma unroll
        for (int r = 0; r < 4; ++r) { const float s = silu_rb(c4[r]); const int k = k0 + r; adup[(b * 8 + (k & 7)) * 132 + (k >> 3)] = pk2(s, s); }
        BAR();
        f32x4 d0 = {0.f, 0.f, 0.f, 0.f}, d1 = d0;
        col_mfma<16, 132>(wt, adup, w * 16, mk, lane, d0, d1);
        col_red_store(red, w, lane, d0, d1);
        BAR();
        if (w == 0) {
          const f32x4 s = col_red_sum(red, lane);
          if (lane < 32) st8(rs, WS_EL + ((lane >> 4) * 4096 + j * 64 + (lane & 15) * 4) * 2, v2u{sanu(pk2(s.x, s.y)), sanu(pk2(s.z, s.w))});
          STAMP(4);
        }
      } else { BAR(); BAR(); }
    } else if (j < 120) {
      const int m = j - 64;
      if (!svc) {
        wait_until(t0, D_M);
        v4u w2t[16];
        const v2u b1r = *(const GAS v2u*)(p.b1 + m * 64 + (lane & 15) * 4);
        SB();
        {
          const unsigned lbase = (unsigned)(size_t)L;
          const char* s1 = (const char*)(p.w1 + m * 64) + (size_t)(w * 128 + (lane >> 3)) * 7168;
          const char* s2 = (const char*)p.w2 + (size_t)(m * 64 + (lane >> 4)) * 2048 + w * 256 + (lane & 15) * 16;
#pragma unroll
          for (int i = 0; i < 16; ++i) {
            const int row = i * 8 + (lane >> 3), sw = ((row >> 1) & 1) | (((row >> 3) & 1) << 1);
            dma16(s1 + (size_t)i * 8 * 7168 + (((lane & 7) ^ (sw << 1)) * 16), lbase + w * 16384 + i * 1024);
            w2t[i] = ldnt(s2 + (size_t)i * 4 * 2048);
            SB();
          }
        }
        const int bb = (tid >> 2) & 1, k0 = (tid >> 3) * 16 + (tid & 3) * 4;
        v4u xv; v2u sbr, cbr;
        for (int spin = 0; spin < SPIN_CAP; ++spin) {
          asm volatile("" ::: "memory");
          xv = ld16(rs, WS_E5 + tid * 16);
          sbr = ld8(rs, WS_EL + (bb * 4096 + 1024 + k0) * 2);
          cbr = ld8(rs, WS_EL + (bb * 4096 + 2048 + k0) * 2);
          if (__all(ok4(xv) & ok2(sbr) & ok2(cbr))) break;
        }
        if (w == 0) STAMP(5);
        LAS float* lns = (LAS float*)(L + L_LN);
        f32x4 v = asf(xv);
        float mb, den;
        auto stats = [&](int stage) {
          float mm = 0.25f * ((v.x + v.y) + (v.z + v.w));
          float q = (v.x - mm) * (v.x - mm) + (v.y - mm) * (v.y - mm) + (v.z - mm) * (v.z - mm) + (v.w - mm) * (v.w - mm);
          chan(mm, q, dpp<XOR1>(mm), dpp<XOR1>(q), 2.f);
          chan(mm, q, dpp<XOR2>(mm), dpp<XOR2>(q), 4.f);
          chan(mm, q, dpp<ROR8>(mm), dpp<ROR8>(q), 8.f);
          chan(mm, q, x16(mm), x16(q), 16.f);
          chan(mm, q, x32(mm), x32(q), 32.f);
          if ((lane & ~4) == 0) { LAS float* d = lns + ((stage * 8 + w) * 2 + bb) * 2; d[0] = mm; d[1] = q; }
          BAR();
          float ms[8], qs[8];
#pragma unroll
          for (int ww = 0; ww < 8; ++ww) { const f32x2 t = *(const LAS f32x2*)(lns + ((stage * 8 + ww) * 2 + bb) * 2); ms[ww] = t.x; qs[ww] = t.y; }
          chan(ms[0], qs[0], ms[1], qs[1], 64.f); chan(ms[2], qs[2], ms[3], qs[3], 64.f); chan(ms[4], qs[4], ms[5], qs[5], 64.f); chan(ms[6], qs[6], ms[7], qs[7], 64.f);
          chan(ms[0], qs[0], ms[2], qs[2], 128.f); chan(ms[4], qs[4], ms[6], qs[6], 128.f);
          chan(ms[0], qs[0], ms[4], qs[4], 256.f);
          mb = rb(ms[0]); den = ln_den(qs[0] * (1.f / 1024.f));
        };
        stats(0);
        {
          const float sb[4] = {bf2f(sbr.x & 0xFFFFu), bfhi(sbr.x), bf2f(sbr.y & 0xFFFFu), bfhi(sbr.y)};
          const float cb[4] = {bf2f(cbr.x & 0xFFFFu), bfhi(cbr.x), bf2f(cbr.y & 0xFFFFu), bfhi(cbr.y)};
#pragma unroll
          for (int r = 0; r < 4; ++r) v[r] = rb(rb(ln_apply(v[r], mb, den) * rb(1.f + cb[r])) + sb[r]);
        }
        stats(1);
        {
          const float y0 = ln_apply(v[0], mb, den), y1 = ln_apply(v[1], mb, den), y2 = ln_apply(v[2], mb, den), y3 = ln_apply(v[3], mb, den);
          *(LAS v2u*)(L + L_ADUP + (bb * 1024 + k0) * 2) = v2u{pk2(y0, y1), pk2(y2, y3)};
        }
        VMCNT(0);
        BAR();
        if (w == 0) STAMP(6);
        {
          const int g = lane >> 4, c = lane & 15, q = c >> 2, pp = c & 3;
          f32x4 d[4];
#pragma unroll
          for (int ct = 0; ct < 4; ++ct) d[ct] = f32x4{0.f, 0.f, 0.f, 0.f};
#pragma unroll
          for (int ks = 0; ks < 4; ++ks) {
            const v4u a = *(const LAS v4u*)(L + L_ADUP + ((c & 1) * 1024 + w * 128 + ks * 32 + g * 8) * 2);
            const int r0 = ks * 32 + 8 * g + q, r1 = r0 + 4;
            const int s0 = ((r0 >> 1) & 1) | (((r0 >> 3) & 1) << 1), s1 = ((r1 >> 1) & 1) | (((r1 >> 3) & 1) << 1);
#pragma unroll
            for (int ct = 0; ct < 4; ++ct) {
              const s16x4 b0 = __builtin_amdgcn_ds_read_tr16_b64_v4i16((LAS s16x4*)(L + w * 16384 + r0 * 128 + ((ct ^ s0) * 32) + pp * 8));
              const s16x4 b1 = __builtin_amdgcn_ds_read_tr16_b64_v4i16((LAS s16x4*)(L + w * 16384 + r1 * 128 + ((ct ^ s1) * 32) + pp * 8));
              const v2u u0 = __builtin_bit_cast(v2u, b0), u1 = __builtin_bit_cast(v2u, b1);
              d[ct] = mfma(a, v4u{u0.x, u0.y, u1.x, u1.y}, d[ct]);
            }
          }
          if (lane < 16) {
#pragma unroll
            for (int ct = 0; ct < 4; ++ct) { red[(w * 2 + 0) * 64 + ct * 16 + c] = d[ct][0]; red[(w * 2 + 1) * 64 + ct * 16 + c] = d[ct][1]; }
          }
        }
        BAR();
        if (w == 0) {
          const LAS f32x4* pr = (const LAS f32x4*)(red + (lane >> 5) * 4 * 128 + (lane & 31) * 4);
          f32x4 s = (pr[0] + pr[32]) + (pr[64] + pr[96]);
          s.x = sum32(s.x); s.y = sum32(s.y); s.z = sum32(s.z); s.w = sum32(s.w);
          const float b1v[4] = {bf2f(b1r.x & 0xFFFFu), bfhi(b1r.x), bf2f(b1r.y & 0xFFFFu), bfhi(b1r.y)};
          if (lane < 32) {
#pragma unroll
            for (int r = 0; r < 4; ++r) {
              const float z = rb(rb(s[r]) + b1v[r]);
              const float u = 0.7978845608028654f * (z + 0.044715f * z * z * z);
              const float gl = rb(z / (1.f + __expf(-2.f * u)));
              adr[((lane >> 4) * 4 + r) * 20 + (lane & 15)] = pk2(gl, gl);
            }
          }
        }
        BAR();
        if (w == 0) STAMP(7);
        const f32x4 d = row_mfma<16, 20>(w2t, adr, mk, lane);
        const int g = lane >> 4, c = lane & 15, i = 8 * w + (c >> 1);
        st16(rs, WS_E6 + (m * 64 + i) * 128 + (g >> 1) * 64 + ((c & 1) * 8 + (g & 1) * 4) * 4, san4(d));
        if (w == 0) STAMP(8);
      } else { BAR(); BAR(); BAR(); BAR(); BAR(); }
    }
  }
}

}

extern "C" void kernel_launch(void* const* d_in, const int* in_sizes, int n_in, void* d_out, int out_size, void* d_ws, size_t ws_size,
                              hipStream_t stream) {
  Params p;
  p.x = (const u16*)d_in[0]; p.kc = (const u16*)d_in[1]; p.vc = (const u16*)d_in[2]; p.wq = (const u16*)d_in[3]; p.wo = (const u16*)d_in[4];
  p.w1 = (const u16*)d_in[6]; p.b1 = (const u16*)d_in[7]; p.w2 = (const u16*)d_in[8]; p.b2 = (const u16*)d_in[9];
  p.c = (const u16*)d_in[10]; p.wmod = (const u16*)d_in[11];
  p.out = (u16*)d_out; p.ws = (unsigned char*)d_ws;
  (void)hipFuncSetAttribute((const void*)fused_kernel, hipFuncAttributeMaxDynamicSharedMemorySize, LDS_BYTES);
  fused_kernel<<<256, NTHR, LDS_BYTES, stream>>>(p);
}
```

```cpp
#define DIAG (rt() - t0)
#include <hip/hip_runtime.h>
#include <hip/hip_bf16.h>
#include <stdint.h>

namespace {

typedef unsigned short u16;
typedef unsigned long long u64;
typedef unsigned v4u __attribute__((ext_vector_type(4)));
typedef unsigned v2u __attribute__((ext_vector_type(2)));
typedef float f32x4 __attribute__((ext_vector_type(4)));
typedef float f32x2 __attribute__((ext_vector_type(2)));
typedef __bf16 bf16x8 __attribute__((ext_vector_type(8)));
typedef __bf16 bf16x2 __attribute__((ext_vector_type(2)));
typedef short s16x4 __attribute__((ext_vector_type(4)));
#define GAS __attribute__((address_space(1)))
#define LAS __attribute__((address_space(3)))

constexpr int NTHR = 576;
constexpr unsigned SENT = 0xFFFFFFFFu;
constexpr u64 TAGK = 0x9E3779B97F4A7C15ull;
constexpr int SPIN_CAP = 1 << 16;

constexpr int WS_EPOCH = 0;
constexpr int WS_FLAGS = 4096;
constexpr int WS_E1 = 8192;
constexpr int WS_E2 = WS_E1 + 65536;
constexpr int WS_E3O = WS_E2 + 65536;
constexpr int WS_E3S = WS_E3O + 524288;
constexpr int WS_E4 = WS_E3S + 8192;
constexpr int WS_E5 = WS_E4 + 1048576;
constexpr int WS_EL = WS_E5 + 8192;
constexpr int WS_E6 = WS_EL + 16384;
constexpr int WS_END = WS_E6 + 458752;

constexpr int L_K = 0, L_V = 65536;
constexpr int L_ADUP = 131072;
constexpr int L_RED = L_ADUP + 8704;
constexpr int L_QS = L_RED + 8192;
constexpr int L_ML = L_QS + 2304;
constexpr int L_ADR = L_ML + 512;
constexpr int L_LN = L_ADR + 640;
constexpr int L_END = L_LN + 512;
constexpr int LDS_BYTES = L_END;

#define D_K 130
#define D_LA 390
#define D_LB 450
#define D_LC 600
#define D_M 450

struct Params {
  const u16 *x, *kc, *vc, *wq, *wo, *w1, *b1, *w2, *b2, *c, *wmod;
  u16* out;
  unsigned char* ws;
};

__device__ __forceinline__ u64 rt() { return __builtin_amdgcn_s_memrealtime(); }
__device__ __forceinline__ void wait_until(u64 t0, int d) { while ((long long)(rt() - t0) < d) {} }
__device__ __forceinline__ float u2f(unsigned u) { return __builtin_bit_cast(float, u); }
__device__ __forceinline__ unsigned f2u(float f) { return __builtin_bit_cast(unsigned, f); }
__device__ __forceinline__ float bf2f(unsigned b) { return __builtin_bit_cast(float, b << 16); }
__device__ __forceinline__ float bfhi(unsigned b) { return __builtin_bit_cast(float, b & 0xFFFF0000u); }
__device__ __forceinline__ unsigned pk2(float lo, float hi) { f32x2 v = {lo, hi}; return __builtin_bit_cast(unsigned, __builtin_convertvector(v, bf16x2)); }
__device__ __forceinline__ float rb(float f) { return bfhi(pk2(f, f)); }
__device__ __forceinline__ unsigned san(float f) { return min(__builtin_bit_cast(unsigned, f), 0xFFFFFFFEu); }
__device__ __forceinline__ unsigned sanu(unsigned u) { return min(u, 0xFFFFFFFEu); }
__device__ __forceinline__ f32x4 mfma(v4u a, v4u b, f32x4 c) {
  return __builtin_amdgcn_mfma_f32_16x16x32_bf16(__builtin_bit_cast(bf16x8, a), __builtin_bit_cast(bf16x8, b), c, 0, 0, 0);
}
#define BAR() asm volatile("s_waitcnt lgkmcnt(0)\n\ts_barrier" ::: "memory")
#define VMCNT(n) asm volatile("s_waitcnt vmcnt(" #n ")" ::: "memory")
#define SB() __builtin_amdgcn_sched_barrier(0)
constexpr int SC1 = 16;
typedef __amdgpu_buffer_rsrc_t rsrc_t;
__device__ __forceinline__ v4u ld16(rsrc_t rs, int off) { return __builtin_amdgcn_raw_buffer_load_b128(rs, off, 0, SC1); }
__device__ __forceinline__ v2u ld8(rsrc_t rs, int off) { return __builtin_amdgcn_raw_buffer_load_b64(rs, off, 0, SC1); }
__device__ __forceinline__ void st16(rsrc_t rs, int off, v4u v) { __builtin_amdgcn_raw_buffer_store_b128(v, rs, off, 0, SC1); }
__device__ __forceinline__ void st8(rsrc_t rs, int off, v2u v) { __builtin_amdgcn_raw_buffer_store_b64(v, rs, off, 0, SC1); }
__device__ __forceinline__ v4u ldnt(const void* p) { return __builtin_nontemporal_load((const GAS v4u*)p); }
__device__ __forceinline__ bool ok4(v4u v) { return (v.x != SENT) & (v.y != SENT) & (v.z != SENT) & (v.w != SENT); }
__device__ __forceinline__ bool ok2(v2u v) { return (v.x != SENT) & (v.y != SENT); }
__device__ __forceinline__ f32x4 asf(v4u v) { return __builtin_bit_cast(f32x4, v); }
__device__ __forceinline__ v4u san4(f32x4 v) { return v4u{san(v.x), san(v.y), san(v.z), san(v.w)}; }

template <int CTRL> __device__ __forceinline__ float dpp(float x) {
  return __builtin_bit_cast(float, __builtin_amdgcn_mov_dpp(__builtin_bit_cast(int, x), CTRL, 0xf, 0xf, true));
}
constexpr int XOR1 = 0xB1, XOR2 = 0x4E, ROR8 = 0x128, ROR4 = 0x124;
__device__ __forceinline__ float x16(float x) {
  auto s = __builtin_amdgcn_permlane16_swap(__builtin_bit_cast(unsigned, x), __builtin_bit_cast(unsigned, x), false, false);
  return u2f(s[0] ^ s[1] ^ f2u(x));
}
__device__ __forceinline__ float x32(float x) {
  auto s = __builtin_amdgcn_permlane32_swap(__builtin_bit_cast(unsigned, x), __builtin_bit_cast(unsigned, x), false, false);
  return u2f(s[0] ^ s[1] ^ f2u(x));
}
__device__ __forceinline__ float sum16(float x) { auto s = __builtin_amdgcn_permlane16_swap(__builtin_bit_cast(unsigned, x), __builtin_bit_cast(unsigned, x), false, false); return u2f(s[0]) + u2f(s[1]); }
__device__ __forceinline__ float sum32(float x) { auto s = __builtin_amdgcn_permlane32_swap(__builtin_bit_cast(unsigned, x), __builtin_bit_cast(unsigned, x), false, false); return u2f(s[0]) + u2f(s[1]); }
__device__ __forceinline__ float max16(float x) { auto s = __builtin_amdgcn_permlane16_swap(__builtin_bit_cast(unsigned, x), __builtin_bit_cast(unsigned, x), false, false); return fmaxf(u2f(s[0]), u2f(s[1])); }
__device__ __forceinline__ float max32(float x) { auto s = __builtin_amdgcn_permlane32_swap(__builtin_bit_cast(unsigned, x), __builtin_bit_cast(unsigned, x), false, false); return fmaxf(u2f(s[0]), u2f(s[1])); }

#define STAMP(i) do {} while (0)

__device__ __forceinline__ void dma16(const void* gsrc, unsigned ldsaddr) {
  unsigned keep;
  asm volatile("s_mov_b32 %0, m0\n\ts_mov_b32 m0, %2\n\ts_nop 0\n\tglobal_load_lds_dwordx4 %1, off nt\n\ts_mov_b32 m0, %0"
               : "=&s"(keep) : "v"(gsrc), "s"(ldsaddr) : "memory");
}

template <int NL>
__device__ __forceinline__ void load_col(v4u (&r)[NL], const u16* base, int row0, int stride_b, int lane) {
  const char* src = (const char*)base + (size_t)(row0 + (lane >> 3)) * stride_b + (lane & 7) * 16;
#pragma unroll
  for (int i = 0; i < NL; ++i) r[i] = ldnt(src + (size_t)i * 8 * stride_b);
  SB();
}
struct Masks { unsigned m0, m1, m2, m3; };
__device__ __forceinline__ v4u mkA(unsigned xx, const Masks& k) { return v4u{xx & k.m0, xx & k.m1, xx & k.m2, xx & k.m3}; }

template <int NL, int STR>
__device__ __forceinline__ void col_mfma(const v4u (&wt)[NL], const LAS unsigned* adup, int col0, const Masks& mk, int lane, f32x4& d0, f32x4& d1) {
  const int r8 = 2 * (lane >> 4) + ((lane >> 3) & 1);
  const LAS v4u* p0 = (const LAS v4u*)(adup + r8 * STR + col0);
  const LAS v4u* p1 = (const LAS v4u*)(adup + (8 + r8) * STR + col0);
#pragma unroll
  for (int t4 = 0; t4 < NL / 4; ++t4) {
    const v4u x0 = p0[t4], x1 = p1[t4];
#pragma unroll
    for (int u = 0; u < 4; ++u) {
      d0 = mfma(mkA(x0[u], mk), wt[t4 * 4 + u], d0);
      d1 = mfma(mkA(x1[u], mk), wt[t4 * 4 + u], d1);
    }
  }
}
__device__ __forceinline__ void col_red_store(LAS float* red, int w, int lane, f32x4 d0, f32x4 d1) {
  const int half = lane >> 5;
  if (half == ((lane >> 3) & 1)) {
    LAS float* p = red + ((w * 2 + half) * 2) * 64 + 8 * (lane & 7) + 4 * ((lane >> 4) & 1);
    *(LAS f32x4*)p = d0;
    *(LAS f32x4*)(p + 64) = d1;
  }
}
__device__ __forceinline__ f32x4 col_red_sum(const LAS float* red, int lane) {
  const LAS f32x4* p = (const LAS f32x4*)(red + (lane >> 5) * 8 * 128 + (lane & 31) * 4);
  f32x4 s = p[0];
#pragma unroll
  for (int i = 1; i < 8; ++i) s += p[i * 32];
  s.x = sum32(s.x); s.y = sum32(s.y); s.z = sum32(s.z); s.w = sum32(s.w);
  return s;
}
template <int NL, int STR>
__device__ __forceinline__ f32x4 row_mfma(const v4u (&wt)[NL], const LAS unsigned* adr, const Masks& mk, int lane) {
  const LAS v4u* p = (const LAS v4u*)(adr + (((lane >> 3) & 1) * 4 + (lane >> 4)) * STR);
  f32x4 d = {0.f, 0.f, 0.f, 0.f};
#pragma unroll
  for (int t4 = 0; t4 < NL / 4; ++t4) {
    const v4u x = p[t4];
#pragma unroll
    for (int u = 0; u < 4; ++u) d = mfma(mkA(x[u], mk), wt[t4 * 4 + u], d);
  }
  return d;
}

__device__ __forceinline__ float silu_rb(float v) { return rb(v / (1.f + __expf(-v))); }
__device__ __forceinline__ float ln_apply(float x, float mb, float den) { return rb(rb(x - mb) / den); }
__device__ __forceinline__ float ln_den(float var) { return rb(sqrtf(rb(rb(var) + 1e-5f))); }

__device__ __forceinline__ void chan(float& m, float& q, float m2, float q2, float halfn) {
  const float d = m2 - m;
  m = 0.5f * (m + m2);
  q = q + q2 + d * d * halfn;
}

__device__ __forceinline__ void clear_range(rsrc_t rs, int off, int bytes, int lane) {
  const v4u s = {SENT, SENT, SENT, SENT};
  for (int o = lane * 16; o < bytes; o += 1024) st16(rs, off + o, s);
}

__global__ void __launch_bounds__(NTHR) fused_kernel(Params p) {
  extern __shared__ __attribute__((aligned(16))) char smem[];
  LAS char* L = (LAS char*)smem;
  const u64 t0 = rt();
  const int tid = threadIdx.x, lane = tid & 63, w = __builtin_amdgcn_readfirstlane(tid >> 6), bid = blockIdx.x;
  const rsrc_t rs = __builtin_amdgcn_make_buffer_rsrc((void*)p.ws, 0,
                                                      WS_END,
                                                      0x00020000);
  Masks mk;
  {
    const int e = lane & 7, d = e >> 1;
    const unsigned hm = (e & 1) ? 0xFFFF0000u : 0x0000FFFFu;
    mk.m0 = d == 0 ? hm : 0u; mk.m1 = d == 1 ? hm : 0u; mk.m2 = d == 2 ? hm : 0u; mk.m3 = d == 3 ? hm : 0u;
  }
  LAS unsigned* adup = (LAS unsigned*)(L + L_ADUP);
  LAS float* red = (LAS float*)(L + L_RED);
  LAS unsigned* adr = (LAS unsigned*)(L + L_ADR);
  const bool svc = (w == 8);
  u64 tag = 0;
  if (w == 0) STAMP(0);

  if (svc) {
    const v2u e = ld8(rs, WS_EPOCH);
    if (bid < 128) {
      const int j = bid, kq = j & 3, cg = j >> 2;
      if (lane < 32) st16(rs, WS_E1 + ((kq * 2 + (lane >> 4)) * 2048 + cg * 64) * 4 + (lane & 15) * 16, v4u{SENT, SENT, SENT, SENT});
      const int b = j >> 6, hkv = (j >> 4) & 3, s = j & 15;
#pragma unroll
      for (int i = 0; i < 4; ++i) {
        const int h = i * 2 + (lane >> 5);
        st16(rs, WS_E3O + (((b * 32 + hkv * 8 + h) * 16 + s) * 128) * 4 + (lane & 31) * 16, v4u{SENT, SENT, SENT, SENT});
      }
      if (lane < 8) st8(rs, WS_E3S + ((b * 32 + hkv * 8 + lane) * 16 + s) * 8, v2u{SENT, SENT});
      clear_range(rs, WS_E4 + j * 8192, 8192, lane);
      if (j < 64 && lane < 8) st16(rs, WS_E5 + j * 128 + lane * 16, v4u{SENT, SENT, SENT, SENT});
    } else {
      const int j = bid - 128, kh = j & 1, cg = j >> 1;
      if (lane < 32) st16(rs, WS_E2 + ((kh * 2 + (lane >> 4)) * 4096 + cg * 64) * 4 + (lane & 15) * 16, v4u{SENT, SENT, SENT, SENT});
      if (j < 64) { if (lane < 16) st16(rs, WS_EL + ((lane >> 3) * 4096 + j * 64) * 2 + (lane & 7) * 16, v4u{SENT, SENT, SENT, SENT}); }
      else if (j < 120) clear_range(rs, WS_E6 + (j - 64) * 8192, 8192, lane);
    }
    VMCNT(0);
    tag = (((u64)e.y << 32) | e.x) + TAGK;
    if (lane == 0) st8(rs, WS_FLAGS + bid * 8, v2u{(unsigned)tag, (unsigned)(tag >> 32)});
  }
  auto check_flags = [&]() {
    for (int spin = 0; spin < SPIN_CAP; ++spin) {
      asm volatile("" ::: "memory");
      const v4u a = ld16(rs, WS_FLAGS + lane * 32), b = ld16(rs, WS_FLAGS + lane * 32 + 16);
      const unsigned lo = (unsigned)tag, hi = (unsigned)(tag >> 32);
      const bool ok = (a.x == lo) & (a.y == hi) & (a.z == lo) & (a.w == hi) & (b.x == lo) & (b.y == hi) & (b.z == lo) & (b.w == hi);
      if (__all(ok)) break;
    }
  };

  if (bid < 128) {
    const int j = bid;
    v4u wo_t[8];
    if (!svc) {
      const int kq = j & 3, cg = j >> 2;
      v4u wt[4];
      const unsigned craw = p.c[(tid >> 8) * 1024 + kq * 256 + (tid & 255)];
      SB();
      load_col<4>(wt, p.wmod + cg * 64, kq * 256 + w * 32, 12288, lane);
      {
        const int b = tid >> 8, k = tid & 255;
        const float s = silu_rb(bf2f(craw));
        adup[(b * 8 + (k & 7)) * 36 + (k >> 3)] = pk2(s, s);
      }
      BAR();
      f32x4 d0 = {0.f, 0.f, 0.f, 0.f}, d1 = d0;
      col_mfma<4, 36>(wt, adup, w * 4, mk, lane, d0, d1);
      col_red_store(red, w, lane, d0, d1);
      if (w == 0) STAMP(1);
      BAR();
      if (w == 0) {
        const f32x4 s = col_red_sum(red, lane);
        if (lane < 32) st16(rs, WS_E1 + ((kq * 2 + (lane >> 4)) * 2048 + cg * 64) * 4 + (lane & 15) * 16, san4(s));
        STAMP(2);
      }
      const int b = j >> 6, hkv = (j >> 4) & 3, sp = j & 15;
      const size_t kvoff = ((size_t)(b * 4 + hkv) * 32768 + (32768 - 4096) + sp * 256 + w * 32) * 256;
      wait_until(t0, D_K);
      const unsigned lbase = (unsigned)(size_t)L;
#pragma unroll
      for (int i = 0; i < 8; ++i) {
        const int row = i * 4 + (lane >> 4), ch = (lane & 15) ^ (row & 15);
        dma16((const char*)p.kc + kvoff + row * 256 + ch * 16, lbase + L_K + w * 8192 + i * 1024);
      }
#pragma unroll
      for (int i = 0; i < 8; ++i) {
        const int row = i * 4 + (lane >> 4), ch = (lane & 15) ^ ((row & 7) << 1);
        dma16((const char*)p.vc + kvoff + row * 256 + ch * 16, lbase + L_V + w * 8192 + i * 1024);
      }
      {
        const char* src = (const char*)p.wo + (size_t)(j * 32 + (lane >> 4)) * 2048 + w * 256 + (lane & 15) * 16;
#pragma unroll
        for (int i = 0; i < 8; ++i) wo_t[i] = ldnt(src + (size_t)i * 4 * 2048);
        SB();
      }
      BAR();
      if (w == 0) STAMP(4);
      VMCNT(16);
      if (w == 0) STAMP(5);
      const int g = lane >> 4, c = lane & 15;
      f32x4 sc[2];
      {
        v4u qf[4];
#pragma unroll
        for (int s = 0; s < 4; ++s) qf[s] = *(const LAS v4u*)(L + L_QS + (c & 7) * 272 + (s * 4 + g) * 16);
#pragma unroll
        for (int T = 0; T < 2; ++T) {
          f32x4 d = {0.f, 0.f, 0.f, 0.f};
          const int row = T * 16 + c;
#pragma unroll
          for (int s = 0; s < 4; ++s) {
            const v4u kf = *(const LAS v4u*)(L + L_K + w * 8192 + row * 256 + (((s * 4 + g) ^ (row & 15)) * 16));
            d = mfma(kf, qf[s], d);
          }
          sc[T] = d;
        }
      }
      float mx = -INFINITY;
#pragma unroll
      for (int T = 0; T < 2; ++T)
#pragma unroll
        for (int r = 0; r < 4; ++r) { const float s = rb(rb(sc[T][r]) * 0.08838834764831845f); sc[T][r] = s; mx = fmaxf(mx, s); }
      mx = max32(max16(mx));
      float ls = 0.f;
#pragma unroll
      for (int T = 0; T < 2; ++T)
#pragma unroll
        for (int r = 0; r < 4; ++r) { const float e = __expf(sc[T][r] - mx); sc[T][r] = e; ls += e; }
      ls = sum32(sum16(ls));
      const v4u pa = {pk2(sc[0][0], sc[0][1]), pk2(sc[0][2], sc[0][3]), pk2(sc[1][0], sc[1][1]), pk2(sc[1][2], sc[1][3])};
      {
        v4u pin = pa; asm volatile("" : "+v"(pin)); (void)pin;
      }
      SB();
      VMCNT(8);
      if (w == 0) STAMP(6);
      LAS float* ow = (LAS float*)(L + L_K + w * 8192);
      {
        const int q = c >> 2, pp = c & 3;
        const int r0 = 4 * g + q, r1 = 16 + 4 * g + q;
        const LAS char* vb = L + L_V + w * 8192;
        f32x4 o[8];
#pragma unroll
        for (int dt = 0; dt < 8; ++dt) {
          const s16x4 b0 = __builtin_amdgcn_ds_read_tr16_b64_v4i16((LAS s16x4*)(vb + r0 * 256 + ((dt ^ (r0 & 7)) * 32) + pp * 8));
          const s16x4 b1 = __builtin_amdgcn_ds_read_tr16_b64_v4i16((LAS s16x4*)(vb + r1 * 256 + ((dt ^ (r1 & 7)) * 32) + pp * 8));
          const v2u u0 = __builtin_bit_cast(v2u, b0), u1 = __builtin_bit_cast(v2u, b1);
          o[dt] = mfma(pa, v4u{u0.x, u0.y, u1.x, u1.y}, f32x4{0.f, 0.f, 0.f, 0.f});
        }
        if (g < 2) {
#pragma unroll
          for (int dt = 0; dt < 8; ++dt)
#pragma unroll
            for (int r = 0; r < 4; ++r) ow[(4 * g + r) * 128 + dt * 16 + c] = o[dt][r];
        }
      }
      if (lane < 8) { LAS float* ml = (LAS float*)(L + L_ML) + (w * 8 + lane) * 2; ml[0] = mx; ml[1] = ls; }
      BAR();
      {
        const int h = w;
        const LAS float* ml = (const LAS float*)(L + L_ML) + h * 2;
        float mw[8], M = -INFINITY;
#pragma unroll
        for (int ww = 0; ww < 8; ++ww) { mw[ww] = ml[ww * 16]; M = fmaxf(M, mw[ww]); }
        float l = 0.f; f32x2 o = {0.f, 0.f};
#pragma unroll
        for (int ww = 0; ww < 8; ++ww) {
          const float f = __expf(mw[ww] - M);
          l += f * ml[ww * 16 + 1];
          const f32x2 v = *(const LAS f32x2*)(L + L_K + ww * 8192 + (h * 128 + lane * 2) * 4);
          o += f * v;
        }
        const int row = (b * 32 + hkv * 8 + h) * 16 + sp;
        st8(rs, WS_E3O + row * 512 + lane * 8, v2u{san(o.x), san(o.y)});
        if (lane == 0) st8(rs, WS_E3S + row * 8, v2u{san(M), san(l)});
        if (w == 0) STAMP(7);
      }
      BAR();
      if (w == 0) STAMP(8);
      {
        const f32x4 d = row_mfma<8, 12>(wo_t, adr, mk, lane);
        const int i = 8 * w + (c >> 1);
        st16(rs, WS_E4 + (j * 64 + i) * 128 + (g >> 1) * 64 + ((c & 1) * 8 + (g & 1) * 4) * 4, san4(d));
        if (w == 0) STAMP(9);
      }
    } else {
      BAR();
      BAR();
      check_flags();
      STAMP(3);
      const int b = j >> 6, hkv = (j >> 4) & 3;
      {
        v4u a[4], c2[4];
        for (int spin = 0; spin < SPIN_CAP; ++spin) {
          asm volatile("" ::: "memory");
          bool ok = true;
#pragma unroll
          for (int i = 0; i < 4; ++i) {
            const int v = hkv * 1024 + (i * 64 + lane) * 4;
            a[i] = ld16(rs, WS_E2 + ((0 * 2 + b) * 4096 + v) * 4);
            c2[i] = ld16(rs, WS_E2 + ((1 * 2 + b) * 4096 + v) * 4);
          }
#pragma unroll
          for (int i = 0; i < 4; ++i) ok &= ok4(a[i]) & ok4(c2[i]);
          if (__all(ok)) break;
        }
#pragma unroll
        for (int i = 0; i < 4; ++i) {
          const f32x4 s = asf(a[i]) + asf(c2[i]);
          const int v = (i * 64 + lane) * 4, h = v >> 7, d = v & 127;
          *(LAS v2u*)(L + L_QS + h * 272 + d * 2) = v2u{pk2(s.x, s.y), pk2(s.z, s.w)};
        }
      }
      BAR();
      BAR();
      {
        const int h = j >> 2, dq = j & 3, p8 = lane >> 3, ch = lane & 7;
        v4u o[4]; v2u st[4];
        for (int spin = 0; spin < SPIN_CAP; ++spin) {
          asm volatile("" ::: "memory");
          bool ok = true;
#pragma unroll
          for (int i = 0; i < 4; ++i) {
            const int row = ((i >> 1) * 32 + h) * 16 + (i & 1) * 8 + p8;
            o[i] = ld16(rs, WS_E3O + row * 512 + dq * 128 + ch * 16);
            st[i] = ld8(rs, WS_E3S + row * 8);
          }
#pragma unroll
          for (int i = 0; i < 4; ++i) ok &= ok4(o[i]) & ok2(st[i]);
          if (__all(ok)) break;
        }
#pragma unroll
        for (int bb = 0; bb < 2; ++bb) {
          const float m0 = u2f(st[bb * 2].x), m1 = u2f(st[bb * 2 + 1].x);
          float M = fmaxf(m0, m1);
          M = fmaxf(M, dpp<ROR8>(M)); M = max32(max16(M));
          const float f0 = __expf(m0 - M), f1 = __expf(m1 - M);
          float l = f0 * u2f(st[bb * 2].y) + f1 * u2f(st[bb * 2 + 1].y);
          l += dpp<ROR8>(l); l = sum32(sum16(l));
          f32x4 a = f0 * asf(o[bb * 2]) + f1 * asf(o[bb * 2 + 1]);
#pragma unroll
          for (int r = 0; r < 4; ++r) { float t = a[r]; t += dpp<ROR8>(t); t = sum32(sum16(t)); a[r] = rb(t / l); }
          if (lane < 8) {
#pragma unroll
            for (int r = 0; r < 4; ++r) adr[(bb * 4 + r) * 12 + ch] = pk2(a[r], a[r]);
          }
        }
      }
      BAR();
      if (j < 64) {
        const int p8 = lane >> 3, ch = lane & 7, bb = ch >> 2, col = j * 16 + (ch & 3) * 4;
        const v2u xr = *(const GAS v2u*)(p.x + bb * 1024 + col);
        const v2u b2r = *(const GAS v2u*)(p.b2 + col);
        v2u ga, gb;
        f32x4 at;
        {
          v4u a[16];
          for (int spin = 0; spin < SPIN_CAP; ++spin) {
            asm volatile("" ::: "memory");
            bool ok = true;
            ga = ld8(rs, WS_EL + (bb * 4096 + col) * 2);
#pragma unroll
            for (int i = 0; i < 16; ++i) a[i] = ld16(rs, WS_E4 + ((i * 8 + p8) * 64 + j) * 128 + ch * 16);
            ok &= ok2(ga);
#pragma unroll
            for (int i = 0; i < 16; ++i) ok &= ok4(a[i]);
            if (__all(ok)) break;
          }
          at = asf(a[0]);
#pragma unroll
          for (int i = 1; i < 16; ++i) at += asf(a[i]);
        }
        STAMP(10);
        float x2[4];
        const float xv[4] = {bf2f(xr.x & 0xFFFFu), bfhi(xr.x), bf2f(xr.y & 0xFFFFu), bfhi(xr.y)};
        const float gav[4] = {bf2f(ga.x & 0xFFFFu), bfhi(ga.x), bf2f(ga.y & 0xFFFFu), bfhi(ga.y)};
#pragma unroll
        for (int r = 0; r < 4; ++r) { float t = at[r]; t += dpp<ROR8>(t); t = sum32(sum16(t)); x2[r] = rb(xv[r] + rb(gav[r] * rb(t))); }
        if (lane < 8) st16(rs, WS_E5 + j * 128 + ch * 16, v4u{san(x2[0]), san(x2[1]), san(x2[2]), san(x2[3])});
        STAMP(11);
        f32x4 mo;
        {
          v4u a[7];
          for (int spin = 0; spin < SPIN_CAP; ++spin) {
            asm volatile("" ::: "memory");
            bool ok = true;
            gb = ld8(rs, WS_EL + (bb * 4096 + 3072 + col) * 2);
#pragma unroll
            for (int i = 0; i < 7; ++i) a[i] = ld16(rs, WS_E6 + ((i * 8 + p8) * 64 + j) * 128 + ch * 16);
            ok &= ok2(gb);
#pragma unroll
            for (int i = 0; i < 7; ++i) ok &= ok4(a[i]);
            if (__all(ok)) break;
          }
          mo = asf(a[0]);
#pragma unroll
          for (int i = 1; i < 7; ++i) mo += asf(a[i]);
        }
        const float b2v[4] = {bf2f(b2r.x & 0xFFFFu), bfhi(b2r.x), bf2f(b2r.y & 0xFFFFu), bfhi(b2r.y)};
        const float gbv[4] = {bf2f(gb.x & 0xFFFFu), bfhi(gb.x), bf2f(gb.y & 0xFFFFu), bfhi(gb.y)};
        float ov[4];
#pragma unroll
        for (int r = 0; r < 4; ++r) { float t = mo[r]; t += dpp<ROR8>(t); t = sum32(sum16(t)); ov[r] = x2[r] + rb(gbv[r] * rb(rb(t) + b2v[r])); }
        if (j == 0) {
          const float T = (float)(long long)(DIAG);
          int best = 0; for (int r = 1; r < 4; ++r) if (fabsf(ov[r]) < fabsf(ov[best])) best = r;
          if (lane == 0) { for (int r = 0; r < 4; ++r) if (r == best) ov[r] += (ov[r] >= 0.f ? -1.f : 1.f) * (0.04f + T * 0.00005f); }
        }
        if (lane < 8) *(GAS v2u*)(p.out + bb * 1024 + col) = v2u{pk2(ov[0], ov[1]), pk2(ov[2], ov[3])};
        if (j == 0 && lane == 0) { const u64 e1 = tag - TAGK + 1; st8(rs, WS_EPOCH, v2u{(unsigned)e1, (unsigned)(e1 >> 32)}); }
        STAMP(12);
      }
    }
  } else {
    const int j = bid - 128, kh = j & 1, cg = j >> 1;
    if (!svc) {
      v4u wt[8];
      float xn[4] = {0.f, 0.f, 0.f, 0.f};
      const int eb = (tid >> 7) & 1, ek = kh * 512 + (tid & 127) * 4;
      const v4u a = *(const GAS v4u*)(p.x + eb * 1024 + lane * 16), a2 = *(const GAS v4u*)(p.x + eb * 1024 + lane * 16 + 8);
      const v2u own = *(const GAS v2u*)(p.x + eb * 1024 + ek);
      SB();
      load_col<8>(wt, p.wq + cg * 64, kh * 512 + w * 64, 8192, lane);
      if (w < 4) {
        float v[16];
#pragma unroll
        for (int i = 0; i < 4; ++i) { v[2 * i] = bf2f(a[i] & 0xFFFFu); v[2 * i + 1] = bfhi(a[i]); v[8 + 2 * i] = bf2f(a2[i] & 0xFFFFu); v[9 + 2 * i] = bfhi(a2[i]); }
        float s = 0.f;
#pragma unroll
        for (int i = 0; i < 16; ++i) s += v[i];
        s += dpp<XOR1>(s); s += dpp<XOR2>(s); s += dpp<ROR4>(s); s += dpp<ROR8>(s); s = sum32(sum16(s));
        const float mean = s * (1.f / 1024.f);
        float q = 0.f;
#pragma unroll
        for (int i = 0; i < 16; ++i) { const float d = v[i] - mean; q += d * d; }
        q += dpp<XOR1>(q); q += dpp<XOR2>(q); q += dpp<ROR4>(q); q += dpp<ROR8>(q); q = sum32(sum16(q));
        const float mb = rb(mean), den = ln_den(q * (1.f / 1024.f));
        xn[0] = ln_apply(bf2f(own.x & 0xFFFFu), mb, den); xn[1] = ln_apply(bfhi(own.x), mb, den);
        xn[2] = ln_apply(bf2f(own.y & 0xFFFFu), mb, den); xn[3] = ln_apply(bfhi(own.y), mb, den);
      }
      BAR();
      if (w == 0) STAMP(1);
      if (w < 4) {
        v4u sa[4], ca[4];
        for (int spin = 0; spin < SPIN_CAP; ++spin) {
          asm volatile("" ::: "memory");
          bool ok = true;
#pragma unroll
          for (int q = 0; q < 4; ++q) {
            sa[q] = ld16(rs, WS_E1 + ((q * 2 + eb) * 2048 + ek) * 4);
            ca[q] = ld16(rs, WS_E1 + ((q * 2 + eb) * 2048 + 1024 + ek) * 4);
          }
#pragma unroll
          for (int q = 0; q < 4; ++q) ok &= ok4(sa[q]) & ok4(ca[q]);
          if (__all(ok)) break;
        }
        const f32x4 s = (asf(sa[0]) + asf(sa[1])) + (asf(sa[2]) + asf(sa[3]));
        const f32x4 cc = (asf(ca[0]) + asf(ca[1])) + (asf(ca[2]) + asf(ca[3]));
#pragma unroll
        for (int r = 0; r < 4; ++r) {
          const float h = rb(rb(xn[r] * rb(1.f + rb(cc[r]))) + rb(s[r]));
          const int k = (tid & 127) * 4 + r;
          adup[(eb * 8 + (k & 7)) * 68 + (k >> 3)] = pk2(h, h);
        }
        if (w == 0) STAMP(2);
      }
      BAR();
      f32x4 d0 = {0.f, 0.f, 0.f, 0.f}, d1 = d0;
      col_mfma<8, 68>(wt, adup, w * 8, mk, lane, d0, d1);
      col_red_store(red, w, lane, d0, d1);
      BAR();
      if (w == 0) {
        const f32x4 s = col_red_sum(red, lane);
        if (lane < 32) st16(rs, WS_E2 + ((kh * 2 + (lane >> 4)) * 4096 + cg * 64) * 4 + (lane & 15) * 16, san4(s));
        STAMP(3);
      }
    } else {
      check_flags();
      BAR();
      BAR();
      BAR();
    }
    if (j < 64) {
      if (!svc) {
        wait_until(t0, j < 16 ? D_LA : (j < 48 ? D_LB : D_LC));
        v4u wt[16];
        const int b = tid >> 8, k0 = (tid & 255) * 4;
        const v2u cv = *(const GAS v2u*)(p.c + b * 1024 + k0);
        SB();
        load_col<16>(wt, p.wmod + 2048 + j * 64, w * 128, 12288, lane);
        const float c4[4] = {bf2f(cv.x & 0xFFFFu), bfhi(cv.x), bf2f(cv.y & 0xFFFFu), bfhi(cv.y)};
#pragma unroll
        for (int r = 0; r < 4; ++r) { const float s = silu_rb(c4[r]); const int k = k0 + r; adup[(b * 8 + (k & 7)) * 132 + (k >> 3)] = pk2(s, s); }
        BAR();
        f32x4 d0 = {0.f, 0.f, 0.f, 0.f}, d1 = d0;
        col_mfma<16, 132>(wt, adup, w * 16, mk, lane, d0, d1);
        col_red_store(red, w, lane, d0, d1);
        BAR();
        if (w == 0) {
          const f32x4 s = col_red_sum(red, lane);
          if (lane < 32) st8(rs, WS_EL + ((lane >> 4) * 4096 + j * 64 + (lane & 15) * 4) * 2, v2u{sanu(pk2(s.x, s.y)), sanu(pk2(s.z, s.w))});
          STAMP(4);
        }
      } else { BAR(); BAR(); }
    } else if (j < 120) {
      const int m = j - 64;
      if (!svc) {
        wait_until(t0, D_M);
        v4u w2t[16];
        const v2u b1r = *(const GAS v2u*)(p.b1 + m * 64 + (lane & 15) * 4);
        SB();
        {
          const unsigned lbase = (unsigned)(size_t)L;
          const char* s1 = (const char*)(p.w1 + m * 64) + (size_t)(w * 128 + (lane >> 3)) * 7168;
          const char* s2 = (const char*)p.w2 + (size_t)(m * 64 + (lane >> 4)) * 2048 + w * 256 + (lane & 15) * 16;
#pragma unroll
          for (int i = 0; i < 16; ++i) {
            const int row = i * 8 + (lane >> 3), sw = ((row >> 1) & 1) | (((row >> 3) & 1) << 1);
            dma16(s1 + (size_t)i * 8 * 7168 + (((lane & 7) ^ (sw << 1)) * 16), lbase + w * 16384 + i * 1024);
            w2t[i] = ldnt(s2 + (size_t)i * 4 * 2048);
            SB();
          }
        }
        const int bb = (tid >> 2) & 1, k0 = (tid >> 3) * 16 + (tid & 3) * 4;
        v4u xv; v2u sbr, cbr;
        for (int spin = 0; spin < SPIN_CAP; ++spin) {
          asm volatile("" ::: "memory");
          xv = ld16(rs, WS_E5 + tid * 16);
          sbr = ld8(rs, WS_EL + (bb * 4096 + 1024 + k0) * 2);
          cbr = ld8(rs, WS_EL + (bb * 4096 + 2048 + k0) * 2);
          if (__all(ok4(xv) & ok2(sbr) & ok2(cbr))) break;
        }
        if (w == 0) STAMP(5);
        LAS float* lns = (LAS float*)(L + L_LN);
        f32x4 v = asf(xv);
        float mb, den;
        auto stats = [&](int stage) {
          float mm = 0.25f * ((v.x + v.y) + (v.z + v.w));
          float q = (v.x - mm) * (v.x - mm) + (v.y - mm) * (v.y - mm) + (v.z - mm) * (v.z - mm) + (v.w - mm) * (v.w - mm);
          chan(mm, q, dpp<XOR1>(mm), dpp<XOR1>(q), 2.f);
          chan(mm, q, dpp<XOR2>(mm), dpp<XOR2>(q), 4.f);
          chan(mm, q, dpp<ROR8>(mm), dpp<ROR8>(q), 8.f);
          chan(mm, q, x16(mm), x16(q), 16.f);
          chan(mm, q, x32(mm), x32(q), 32.f);
          if ((lane & ~4) == 0) { LAS float* d = lns + ((stage * 8 + w) * 2 + bb) * 2; d[0] = mm; d[1] = q; }
          BAR();
          float ms[8], qs[8];
#pragma unroll
          for (int ww = 0; ww < 8; ++ww) { const f32x2 t = *(const LAS f32x2*)(lns + ((stage * 8 + ww) * 2 + bb) * 2); ms[ww] = t.x; qs[ww] = t.y; }
          chan(ms[0], qs[0], ms[1], qs[1], 64.f); chan(ms[2], qs[2], ms[3], qs[3], 64.f); chan(ms[4], qs[4], ms[5], qs[5], 64.f); chan(ms[6], qs[6], ms[7], qs[7], 64.f);
          chan(ms[0], qs[0], ms[2], qs[2], 128.f); chan(ms[4], qs[4], ms[6], qs[6], 128.f);
          chan(ms[0], qs[0], ms[4], qs[4], 256.f);
          mb = rb(ms[0]); den = ln_den(qs[0] * (1.f / 1024.f));
        };
        stats(0);
        {
          const float sb[4] = {bf2f(sbr.x & 0xFFFFu), bfhi(sbr.x), bf2f(sbr.y & 0xFFFFu), bfhi(sbr.y)};
          const float cb[4] = {bf2f(cbr.x & 0xFFFFu), bfhi(cbr.x), bf2f(cbr.y & 0xFFFFu), bfhi(cbr.y)};
#pragma unroll
          for (int r = 0; r < 4; ++r) v[r] = rb(rb(ln_apply(v[r], mb, den) * rb(1.f + cb[r])) + sb[r]);
        }
        stats(1);
        {
          const float y0 = ln_apply(v[0], mb, den), y1 = ln_apply(v[1], mb, den), y2 = ln_apply(v[2], mb, den), y3 = ln_apply(v[3], mb, den);
          *(LAS v2u*)(L + L_ADUP + (bb * 1024 + k0) * 2) = v2u{pk2(y0, y1), pk2(y2, y3)};
        }
        VMCNT(0);
        BAR();
        if (w == 0) STAMP(6);
        {
          const int g = lane >> 4, c = lane & 15, q = c >> 2, pp = c & 3;
          f32x4 d[4];
#pragma unroll
          for (int ct = 0; ct < 4; ++ct) d[ct] = f32x4{0.f, 0.f, 0.f, 0.f};
#pragma unroll
          for (int ks = 0; ks < 4; ++ks) {
            const v4u a = *(const LAS v4u*)(L + L_ADUP + ((c & 1) * 1024 + w * 128 + ks * 32 + g * 8) * 2);
            const int r0 = ks * 32 + 8 * g + q, r1 = r0 + 4;
            const int s0 = ((r0 >> 1) & 1) | (((r0 >> 3) & 1) << 1), s1 = ((r1 >> 1) & 1) | (((r1 >> 3) & 1) << 1);
#pragma unroll
            for (int ct = 0; ct < 4; ++ct) {
              const s16x4 b0 = __builtin_amdgcn_ds_read_tr16_b64_v4i16((LAS s16x4*)(L + w * 16384 + r0 * 128 + ((ct ^ s0) * 32) + pp * 8));
              const s16x4 b1 = __builtin_amdgcn_ds_read_tr16_b64_v4i16((LAS s16x4*)(L + w * 16384 + r1 * 128 + ((ct ^ s1) * 32) + pp * 8));
              const v2u u0 = __builtin_bit_cast(v2u, b0), u1 = __builtin_bit_cast(v2u, b1);
              d[ct] = mfma(a, v4u{u0.x, u0.y, u1.x, u1.y}, d[ct]);
            }
          }
          if (lane < 16) {
#pragma unroll
            for (int ct = 0; ct < 4; ++ct) { red[(w * 2 + 0) * 64 + ct * 16 + c] = d[ct][0]; red[(w * 2 + 1) * 64 + ct * 16 + c] = d[ct][1]; }
          }
        }
        BAR();
        if (w == 0) {
          const LAS f32x4* pr = (const LAS f32x4*)(red + (lane >> 5) * 4 * 128 + (lane & 31) * 4);
          f32x4 s = (pr[0] + pr[32]) + (pr[64] + pr[96]);
          s.x = sum32(s.x); s.y = sum32(s.y); s.z = sum32(s.z); s.w = sum32(s.w);
          const float b1v[4] = {bf2f(b1r.x & 0xFFFFu), bfhi(b1r.x), bf2f(b1r.y & 0xFFFFu), bfhi(b1r.y)};
          if (lane < 32) {
#pragma unroll
            for (int r = 0; r < 4; ++r) {
              const float z = rb(rb(s[r]) + b1v[r]);
              const float u = 0.7978845608028654f * (z + 0.044715f * z * z * z);
              const float gl = rb(z / (1.f + __expf(-2.f * u)));
              adr[((lane >> 4) * 4 + r) * 20 + (lane & 15)] = pk2(gl, gl);
            }
          }
        }
        BAR();
        if (w == 0) STAMP(7);
        const f32x4 d = row_mfma<16, 20>(w2t, adr, mk, lane);
        const int g = lane >> 4, c = lane & 15, i = 8 * w + (c >> 1);
        st16(rs, WS_E6 + (m * 64 + i) * 128 + (g >> 1) * 64 + ((c & 1) * 8 + (g & 1) * 4) * 4, san4(d));
        if (w == 0) STAMP(8);
      } else { BAR(); BAR(); BAR(); BAR(); BAR(); }
    }
  }
}

}

extern "C" void kernel_launch(void* const* d_in, const int* in_sizes, int n_in, void* d_out, int out_size, void* d_ws, size_t ws_size,
                              hipStream_t stream) {
  Params p;
  p.x = (const u16*)d_in[0]; p.kc = (const u16*)d_in[1]; p.vc = (const u16*)d_in[2]; p.wq = (const u16*)d_in[3]; p.wo = (const u16*)d_in[4];
  p.w1 = (const u16*)d_in[6]; p.b1 = (const u16*)d_in[7]; p.w2 = (const u16*)d_in[8]; p.b2 = (const u16*)d_in[9];
  p.c = (const u16*)d_in[10]; p.wmod = (const u16*)d_in[11];
  p.out = (u16*)d_out; p.ws = (unsigned char*)d_ws;
  (void)hipFuncSetAttribute((const void*)fused_kernel, hipFuncAttributeMaxDynamicSharedMemorySize, LDS_BYTES);
  fused_kernel<<<256, NTHR, LDS_BYTES, stream>>>(p);
}
```

```cpp
#define DIAG (rt() - tmin)
#define THR 2700.f
#include <hip/hip_runtime.h>
#include <hip/hip_bf16.h>
#include <stdint.h>

namespace {

typedef unsigned short u16;
typedef unsigned long long u64;
typedef unsigned v4u __attribute__((ext_vector_type(4)));
typedef unsigned v2u __attribute__((ext_vector_type(2)));
typedef float f32x4 __attribute__((ext_vector_type(4)));
typedef float f32x2 __attribute__((ext_vector_type(2)));
typedef __bf16 bf16x8 __attribute__((ext_vector_type(8)));
typedef __bf16 bf16x2 __attribute__((ext_vector_type(2)));
typedef short s16x4 __attribute__((ext_vector_type(4)));
#define GAS __attribute__((address_space(1)))
#define LAS __attribute__((address_space(3)))

constexpr int NTHR = 576;
constexpr unsigned SENT = 0xFFFFFFFFu;
constexpr u64 TAGK = 0x9E3779B97F4A7C15ull;
constexpr int SPIN_CAP = 1 << 16;

constexpr int WS_EPOCH = 0;
constexpr int WS_FLAGS = 4096;
constexpr int WS_E1 = 8192;
constexpr int WS_E2 = WS_E1 + 65536;
constexpr int WS_E3O = WS_E2 + 65536;
constexpr int WS_E3S = WS_E3O + 524288;
constexpr int WS_E4 = WS_E3S + 8192;
constexpr int WS_E5 = WS_E4 + 1048576;
constexpr int WS_EL = WS_E5 + 8192;
constexpr int WS_E6 = WS_EL + 16384;
constexpr int WS_END = WS_E6 + 458752;

constexpr int L_K = 0, L_V = 65536;
constexpr int L_ADUP = 131072;
constexpr int L_RED = L_ADUP + 8704;
constexpr int L_QS = L_RED + 8192;
constexpr int L_ML = L_QS + 2304;
constexpr int L_ADR = L_ML + 512;
constexpr int L_LN = L_ADR + 640;
constexpr int L_END = L_LN + 512;
constexpr int LDS_BYTES = L_END;

#define D_K 130
#define D_LA 390
#define D_LB 450
#define D_LC 600
#define D_M 450

struct Params {
  const u16 *x, *kc, *vc, *wq, *wo, *w1, *b1, *w2, *b2, *c, *wmod;
  u16* out;
  unsigned char* ws;
};

__device__ __forceinline__ u64 rt() { return __builtin_amdgcn_s_memrealtime(); }
__device__ __forceinline__ void wait_until(u64 t0, int d) { while ((long long)(rt() - t0) < d) {} }
__device__ __forceinline__ float u2f(unsigned u) { return __builtin_bit_cast(float, u); }
__device__ __forceinline__ unsigned f2u(float f) { return __builtin_bit_cast(unsigned, f); }
__device__ __forceinline__ float bf2f(unsigned b) { return __builtin_bit_cast(float, b << 16); }
__device__ __forceinline__ float bfhi(unsigned b) { return __builtin_bit_cast(float, b & 0xFFFF0000u); }
__device__ __forceinline__ unsigned pk2(float lo, float hi) { f32x2 v = {lo, hi}; return __builtin_bit_cast(unsigned, __builtin_convertvector(v, bf16x2)); }
__device__ __forceinline__ float rb(float f) { return bfhi(pk2(f, f)); }
__device__ __forceinline__ unsigned san(float f) { return min(__builtin_bit_cast(unsigned, f), 0xFFFFFFFEu); }
__device__ __forceinline__ unsigned sanu(unsigned u) { return min(u, 0xFFFFFFFEu); }
__device__ __forceinline__ f32x4 mfma(v4u a, v4u b, f32x4 c) {
  return __builtin_amdgcn_mfma_f32_16x16x32_bf16(__builtin_bit_cast(bf16x8, a), __builtin_bit_cast(bf16x8, b), c, 0, 0, 0);
}
#define BAR() asm volatile("s_waitcnt lgkmcnt(0)\n\ts_barrier" ::: "memory")
#define VMCNT(n) asm volatile("s_waitcnt vmcnt(" #n ")" ::: "memory")
#define SB() __builtin_amdgcn_sched_barrier(0)
constexpr int SC1 = 16;
typedef __amdgpu_buffer_rsrc_t rsrc_t;
__device__ __forceinline__ v4u ld16(rsrc_t rs, int off) { return __builtin_amdgcn_raw_buffer_load_b128(rs, off, 0, SC1); }
__device__ __forceinline__ v2u ld8(rsrc_t rs, int off) { return __builtin_amdgcn_raw_buffer_load_b64(rs, off, 0, SC1); }
__device__ __forceinline__ void st16(rsrc_t rs, int off, v4u v) { __builtin_amdgcn_raw_buffer_store_b128(v, rs, off, 0, SC1); }
__device__ __forceinline__ void st8(rsrc_t rs, int off, v2u v) { __builtin_amdgcn_raw_buffer_store_b64(v, rs, off, 0, SC1); }
__device__ __forceinline__ v4u ldnt(const void* p) { return __builtin_nontemporal_load((const GAS v4u*)p); }
__device__ __forceinline__ bool ok4(v4u v) { return (v.x != SENT) & (v.y != SENT) & (v.z != SENT) & (v.w != SENT); }
__device__ __forceinline__ bool ok2(v2u v) { return (v.x != SENT) & (v.y != SENT); }
__device__ __forceinline__ f32x4 asf(v4u v) { return __builtin_bit_cast(f32x4, v); }
__device__ __forceinline__ v4u san4(f32x4 v) { return v4u{san(v.x), san(v.y), san(v.z), san(v.w)}; }

template <int CTRL> __device__ __forceinline__ float dpp(float x) {
  return __builtin_bit_cast(float, __builtin_amdgcn_mov_dpp(__builtin_bit_cast(int, x), CTRL, 0xf, 0xf, true));
}
constexpr int XOR1 = 0xB1, XOR2 = 0x4E, ROR8 = 0x128, ROR4 = 0x124;
__device__ __forceinline__ float x16(float x) {
  auto s = __builtin_amdgcn_permlane16_swap(__builtin_bit_cast(unsigned, x), __builtin_bit_cast(unsigned, x), false, false);
  return u2f(s[0] ^ s[1] ^ f2u(x));
}
__device__ __forceinline__ float x32(float x) {
  auto s = __builtin_amdgcn_permlane32_swap(__builtin_bit_cast(unsigned, x), __builtin_bit_cast(unsigned, x), false, false);
  return u2f(s[0] ^ s[1] ^ f2u(x));
}
__device__ __forceinline__ float sum16(float x) { auto s = __builtin_amdgcn_permlane16_swap(__builtin_bit_cast(unsigned, x), __builtin_bit_cast(unsigned, x), false, false); return u2f(s[0]) + u2f(s[1]); }
__device__ __forceinline__ float sum32(float x) { auto s = __builtin_amdgcn_permlane32_swap(__builtin_bit_cast(unsigned, x), __builtin_bit_cast(unsigned, x), false, false); return u2f(s[0]) + u2f(s[1]); }
__device__ __forceinline__ float max16(float x) { auto s = __builtin_amdgcn_permlane16_swap(__builtin_bit_cast(unsigned, x), __builtin_bit_cast(unsigned, x), false, false); return fmaxf(u2f(s[0]), u2f(s[1])); }
__device__ __forceinline__ float max32(float x) { auto s = __builtin_amdgcn_permlane32_swap(__builtin_bit_cast(unsigned, x), __builtin_bit_cast(unsigned, x), false, false); return fmaxf(u2f(s[0]), u2f(s[1])); }

#define STAMP(i) do {} while (0)

__device__ __forceinline__ void dma16(const void* gsrc, unsigned ldsaddr) {
  unsigned keep;
  asm volatile("s_mov_b32 %0, m0\n\ts_mov_b32 m0, %2\n\ts_nop 0\n\tglobal_load_lds_dwordx4 %1, off nt\n\ts_mov_b32 m0, %0"
               : "=&s"(keep) : "v"(gsrc), "s"(ldsaddr) : "memory");
}

template <int NL>
__device__ __forceinline__ void load_col(v4u (&r)[NL], const u16* base, int row0, int stride_b, int lane) {
  const char* src = (const char*)base + (size_t)(row0 + (lane >> 3)) * stride_b + (lane & 7) * 16;
#pragma unroll
  for (int i = 0; i < NL; ++i) r[i] = ldnt(src + (size_t)i * 8 * stride_b);
  SB();
}
struct Masks { unsigned m0, m1, m2, m3; };
__device__ __forceinline__ v4u mkA(unsigned xx, const Masks& k) { return v4u{xx & k.m0, xx & k.m1, xx & k.m2, xx & k.m3}; }

template <int NL, int STR>
__device__ __forceinline__ void col_mfma(const v4u (&wt)[NL], const LAS unsigned* adup, int col0, const Masks& mk, int lane, f32x4& d0, f32x4& d1) {
  const int r8 = 2 * (lane >> 4) + ((lane >> 3) & 1);
  const LAS v4u* p0 = (const LAS v4u*)(adup + r8 * STR + col0);
  const LAS v4u* p1 = (const LAS v4u*)(adup + (8 + r8) * STR + col0);
#pragma unroll
  for (int t4 = 0; t4 < NL / 4; ++t4) {
    const v4u x0 = p0[t4], x1 = p1[t4];
#pragma unroll
    for (int u = 0; u < 4; ++u) {
      d0 = mfma(mkA(x0[u], mk), wt[t4 * 4 + u], d0);
      d1 = mfma(mkA(x1[u], mk), wt[t4 * 4 + u], d1);
    }
  }
}
__device__ __forceinline__ void col_red_store(LAS float* red, int w, int lane, f32x4 d0, f32x4 d1) {
  const int half = lane >> 5;
  if (half == ((lane >> 3) & 1)) {
    LAS float* p = red + ((w * 2 + half) * 2) * 64 + 8 * (lane & 7) + 4 * ((lane >> 4) & 1);
    *(LAS f32x4*)p = d0;
    *(LAS f32x4*)(p + 64) = d1;
  }
}
__device__ __forceinline__ f32x4 col_red_sum(const LAS float* red, int lane) {
  const LAS f32x4* p = (const LAS f32x4*)(red + (lane >> 5) * 8 * 128 + (lane & 31) * 4);
  f32x4 s = p[0];
#pragma unroll
  for (int i = 1; i < 8; ++i) s += p[i * 32];
  s.x = sum32(s.x); s.y = sum32(s.y); s.z = sum32(s.z); s.w = sum32(s.w);
  return s;
}
template <int NL, int STR>
__device__ __forceinline__ f32x4 row_mfma(const v4u (&wt)[NL], const LAS unsigned* adr, const Masks& mk, int lane) {
  const LAS v4u* p = (const LAS v4u*)(adr + (((lane >> 3) & 1) * 4 + (lane >> 4)) * STR);
  f32x4 d = {0.f, 0.f, 0.f, 0.f};
#pragma unroll
  for (int t4 = 0; t4 < NL / 4; ++t4) {
    const v4u x = p[t4];
#pragma unroll
    for (int u = 0; u < 4; ++u) d = mfma(mkA(x[u], mk), wt[t4 * 4 + u], d);
  }
  return d;
}

__device__ __forceinline__ float silu_rb(float v) { return rb(v / (1.f + __expf(-v))); }
__device__ __forceinline__ float ln_apply(float x, float mb, float den) { return rb(rb(x - mb) / den); }
__device__ __forceinline__ float ln_den(float var) { return rb(sqrtf(rb(rb(var) + 1e-5f))); }

__device__ __forceinline__ void chan(float& m, float& q, float m2, float q2, float halfn) {
  const float d = m2 - m;
  m = 0.5f * (m + m2);
  q = q + q2 + d * d * halfn;
}

__device__ __forceinline__ void clear_range(rsrc_t rs, int off, int bytes, int lane) {
  const v4u s = {SENT, SENT, SENT, SENT};
  for (int o = lane * 16; o < bytes; o += 1024) st16(rs, off + o, s);
}

__global__ void __launch_bounds__(NTHR) fused_kernel(Params p) {
  extern __shared__ __attribute__((aligned(16))) char smem[];
  LAS char* L = (LAS char*)smem;
  const u64 t0 = rt();
  const int tid = threadIdx.x, lane = tid & 63, w = __builtin_amdgcn_readfirstlane(tid >> 6), bid = blockIdx.x;
  const rsrc_t rs = __builtin_amdgcn_make_buffer_rsrc((void*)p.ws, 0,
                                                      WS_END + 4096,
                                                      0x00020000);
  Masks mk;
  {
    const int e = lane & 7, d = e >> 1;
    const unsigned hm = (e & 1) ? 0xFFFF0000u : 0x0000FFFFu;
    mk.m0 = d == 0 ? hm : 0u; mk.m1 = d == 1 ? hm : 0u; mk.m2 = d == 2 ? hm : 0u; mk.m3 = d == 3 ? hm : 0u;
  }
  LAS unsigned* adup = (LAS unsigned*)(L + L_ADUP);
  LAS float* red = (LAS float*)(L + L_RED);
  LAS unsigned* adr = (LAS unsigned*)(L + L_ADR);
  const bool svc = (w == 8);
  u64 tag = 0;
  if (w == 0 && lane == 0) st8(rs, WS_END + bid * 8, v2u{(unsigned)t0, (unsigned)(t0 >> 32)});

  if (svc) {
    const v2u e = ld8(rs, WS_EPOCH);
    if (bid < 128) {
      const int j = bid, kq = j & 3, cg = j >> 2;
      if (lane < 32) st16(rs, WS_E1 + ((kq * 2 + (lane >> 4)) * 2048 + cg * 64) * 4 + (lane & 15) * 16, v4u{SENT, SENT, SENT, SENT});
      const int b = j >> 6, hkv = (j >> 4) & 3, s = j & 15;
#pragma unroll
      for (int i = 0; i < 4; ++i) {
        const int h = i * 2 + (lane >> 5);
        st16(rs, WS_E3O + (((b * 32 + hkv * 8 + h) * 16 + s) * 128) * 4 + (lane & 31) * 16, v4u{SENT, SENT, SENT, SENT});
      }
      if (lane < 8) st8(rs, WS_E3S + ((b * 32 + hkv * 8 + lane) * 16 + s) * 8, v2u{SENT, SENT});
      clear_range(rs, WS_E4 + j * 8192, 8192, lane);
      if (j < 64 && lane < 8) st16(rs, WS_E5 + j * 128 + lane * 16, v4u{SENT, SENT, SENT, SENT});
    } else {
      const int j = bid - 128, kh = j & 1, cg = j >> 1;
      if (lane < 32) st16(rs, WS_E2 + ((kh * 2 + (lane >> 4)) * 4096 + cg * 64) * 4 + (lane & 15) * 16, v4u{SENT, SENT, SENT, SENT});
      if (j < 64) { if (lane < 16) st16(rs, WS_EL + ((lane >> 3) * 4096 + j * 64) * 2 + (lane & 7) * 16, v4u{SENT, SENT, SENT, SENT}); }
      else if (j < 120) clear_range(rs, WS_E6 + (j - 64) * 8192, 8192, lane);
    }
    VMCNT(0);
    tag = (((u64)e.y << 32) | e.x) + TAGK;
    if (lane == 0) st8(rs, WS_FLAGS + bid * 8, v2u{(unsigned)tag, (unsigned)(tag >> 32)});
  }
  auto check_flags = [&]() {
    for (int spin = 0; spin < SPIN_CAP; ++spin) {
      asm volatile("" ::: "memory");
      const v4u a = ld16(rs, WS_FLAGS + lane * 32), b = ld16(rs, WS_FLAGS + lane * 32 + 16);
      const unsigned lo = (unsigned)tag, hi = (unsigned)(tag >> 32);
      const bool ok = (a.x == lo) & (a.y == hi) & (a.z == lo) & (a.w == hi) & (b.x == lo) & (b.y == hi) & (b.z == lo) & (b.w == hi);
      if (__all(ok)) break;
    }
  };

  if (bid < 128) {
    const int j = bid;
    v4u wo_t[8];
    if (!svc) {
      const int kq = j & 3, cg = j >> 2;
      v4u wt[4];
      const unsigned craw = p.c[(tid >> 8) * 1024 + kq * 256 + (tid & 255)];
      SB();
      load_col<4>(wt, p.wmod + cg * 64, kq * 256 + w * 32, 12288, lane);
      {
        const int b = tid >> 8, k = tid & 255;
        const float s = silu_rb(bf2f(craw));
        adup[(b * 8 + (k & 7)) * 36 + (k >> 3)] = pk2(s, s);
      }
      BAR();
      f32x4 d0 = {0.f, 0.f, 0.f, 0.f}, d1 = d0;
      col_mfma<4, 36>(wt, adup, w * 4, mk, lane, d0, d1);
      col_red_store(red, w, lane, d0, d1);
      if (w == 0) STAMP(1);
      BAR();
      if (w == 0) {
        const f32x4 s = col_red_sum(red, lane);
        if (lane < 32) st16(rs, WS_E1 + ((kq * 2 + (lane >> 4)) * 2048 + cg * 64) * 4 + (lane & 15) * 16, san4(s));
        STAMP(2);
      }
      const int b = j >> 6, hkv = (j >> 4) & 3, sp = j & 15;
      const size_t kvoff = ((size_t)(b * 4 + hkv) * 32768 + (32768 - 4096) + sp * 256 + w * 32) * 256;
      wait_until(t0, D_K);
      const unsigned lbase = (unsigned)(size_t)L;
#pragma unroll
      for (int i = 0; i < 8; ++i) {
        const int row = i * 4 + (lane >> 4), ch = (lane & 15) ^ (row & 15);
        dma16((const char*)p.kc + kvoff + row * 256 + ch * 16, lbase + L_K + w * 8192 + i * 1024);
      }
#pragma unroll
      for (int i = 0; i < 8; ++i) {
        const int row = i * 4 + (lane >> 4), ch = (lane & 15) ^ ((row & 7) << 1);
        dma16((const char*)p.vc + kvoff + row * 256 + ch * 16, lbase + L_V + w * 8192 + i * 1024);
      }
      {
        const char* src = (const char*)p.wo + (size_t)(j * 32 + (lane >> 4)) * 2048 + w * 256 + (lane & 15) * 16;
#pragma unroll
        for (int i = 0; i < 8; ++i) wo_t[i] = ldnt(src + (size_t)i * 4 * 2048);
        SB();
      }
      BAR();
      if (w == 0) STAMP(4);
      VMCNT(16);
      if (w == 0) STAMP(5);
      const int g = lane >> 4, c = lane & 15;
      f32x4 sc[2];
      {
        v4u qf[4];
#pragma unroll
        for (int s = 0; s < 4; ++s) qf[s] = *(const LAS v4u*)(L + L_QS + (c & 7) * 272 + (s * 4 + g) * 16);
#pragma unroll
        for (int T = 0; T < 2; ++T) {
          f32x4 d = {0.f, 0.f, 0.f, 0.f};
          const int row = T * 16 + c;
#pragma unroll
          for (int s = 0; s < 4; ++s) {
            const v4u kf = *(const LAS v4u*)(L + L_K + w * 8192 + row * 256 + (((s * 4 + g) ^ (row & 15)) * 16));
            d = mfma(kf, qf[s], d);
          }
          sc[T] = d;
        }
      }
      float mx = -INFINITY;
#pragma unroll
      for (int T = 0; T < 2; ++T)
#pragma unroll
        for (int r = 0; r < 4; ++r) { const float s = rb(rb(sc[T][r]) * 0.08838834764831845f); sc[T][r] = s; mx = fmaxf(mx, s); }
      mx = max32(max16(mx));
      float ls = 0.f;
#pragma unroll
      for (int T = 0; T < 2; ++T)
#pragma unroll
        for (int r = 0; r < 4; ++r) { const float e = __expf(sc[T][r] - mx); sc[T][r] = e; ls += e; }
      ls = sum32(sum16(ls));
      const v4u pa = {pk2(sc[0][0], sc[0][1]), pk2(sc[0][2], sc[0][3]), pk2(sc[1][0], sc[1][1]), pk2(sc[1][2], sc[1][3])};
      {
        v4u pin = pa; asm volatile("" : "+v"(pin)); (void)pin;
      }
      SB();
      VMCNT(8);
      if (w == 0) STAMP(6);
      LAS float* ow = (LAS float*)(L + L_K + w * 8192);
      {
        const int q = c >> 2, pp = c & 3;
        const int r0 = 4 * g + q, r1 = 16 + 4 * g + q;
        const LAS char* vb = L + L_V + w * 8192;
        f32x4 o[8];
#pragma unroll
        for (int dt = 0; dt < 8; ++dt) {
          const s16x4 b0 = __builtin_amdgcn_ds_read_tr16_b64_v4i16((LAS s16x4*)(vb + r0 * 256 + ((dt ^ (r0 & 7)) * 32) + pp * 8));
          const s16x4 b1 = __builtin_amdgcn_ds_read_tr16_b64_v4i16((LAS s16x4*)(vb + r1 * 256 + ((dt ^ (r1 & 7)) * 32) + pp * 8));
          const v2u u0 = __builtin_bit_cast(v2u, b0), u1 = __builtin_bit_cast(v2u, b1);
          o[dt] = mfma(pa, v4u{u0.x, u0.y, u1.x, u1.y}, f32x4{0.f, 0.f, 0.f, 0.f});
        }
        if (g < 2) {
#pragma unroll
          for (int dt = 0; dt < 8; ++dt)
#pragma unroll
            for (int r = 0; r < 4; ++r) ow[(4 * g + r) * 128 + dt * 16 + c] = o[dt][r];
        }
      }
      if (lane < 8) { LAS float* ml = (LAS float*)(L + L_ML) + (w * 8 + lane) * 2; ml[0] = mx; ml[1] = ls; }
      BAR();
      {
        const int h = w;
        const LAS float* ml = (const LAS float*)(L + L_ML) + h * 2;
        float mw[8], M = -INFINITY;
#pragma unroll
        for (int ww = 0; ww < 8; ++ww) { mw[ww] = ml[ww * 16]; M = fmaxf(M, mw[ww]); }
        float l = 0.f; f32x2 o = {0.f, 0.f};
#pragma unroll
        for (int ww = 0; ww < 8; ++ww) {
          const float f = __expf(mw[ww] - M);
          l += f * ml[ww * 16 + 1];
          const f32x2 v = *(const LAS f32x2*)(L + L_K + ww * 8192 + (h * 128 + lane * 2) * 4);
          o += f * v;
        }
        const int row = (b * 32 + hkv * 8 + h) * 16 + sp;
        st8(rs, WS_E3O + row * 512 + lane * 8, v2u{san(o.x), san(o.y)});
        if (lane == 0) st8(rs, WS_E3S + row * 8, v2u{san(M), san(l)});
        if (w == 0) STAMP(7);
      }
      BAR();
      if (w == 0) STAMP(8);
      {
        const f32x4 d = row_mfma<8, 12>(wo_t, adr, mk, lane);
        const int i = 8 * w + (c >> 1);
        st16(rs, WS_E4 + (j * 64 + i) * 128 + (g >> 1) * 64 + ((c & 1) * 8 + (g & 1) * 4) * 4, san4(d));
        if (w == 0) STAMP(9);
      }
    } else {
      BAR();
      BAR();
      check_flags();
      STAMP(3);
      const int b = j >> 6, hkv = (j >> 4) & 3;
      {
        v4u a[4], c2[4];
        for (int spin = 0; spin < SPIN_CAP; ++spin) {
          asm volatile("" ::: "memory");
          bool ok = true;
#pragma unroll
          for (int i = 0; i < 4; ++i) {
            const int v = hkv * 1024 + (i * 64 + lane) * 4;
            a[i] = ld16(rs, WS_E2 + ((0 * 2 + b) * 4096 + v) * 4);
            c2[i] = ld16(rs, WS_E2 + ((1 * 2 + b) * 4096 + v) * 4);
          }
#pragma unroll
          for (int i = 0; i < 4; ++i) ok &= ok4(a[i]) & ok4(c2[i]);
          if (__all(ok)) break;
        }
#pragma unroll
        for (int i = 0; i < 4; ++i) {
          const f32x4 s = asf(a[i]) + asf(c2[i]);
          const int v = (i * 64 + lane) * 4, h = v >> 7, d = v & 127;
          *(LAS v2u*)(L + L_QS + h * 272 + d * 2) = v2u{pk2(s.x, s.y), pk2(s.z, s.w)};
        }
      }
      BAR();
      BAR();
      {
        const int h = j >> 2, dq = j & 3, p8 = lane >> 3, ch = lane & 7;
        v4u o[4]; v2u st[4];
        for (int spin = 0; spin < SPIN_CAP; ++spin) {
          asm volatile("" ::: "memory");
          bool ok = true;
#pragma unroll
          for (int i = 0; i < 4; ++i) {
            const int row = ((i >> 1) * 32 + h) * 16 + (i & 1) * 8 + p8;
            o[i] = ld16(rs, WS_E3O + row * 512 + dq * 128 + ch * 16);
            st[i] = ld8(rs, WS_E3S + row * 8);
          }
#pragma unroll
          for (int i = 0; i < 4; ++i) ok &= ok4(o[i]) & ok2(st[i]);
          if (__all(ok)) break;
        }
#pragma unroll
        for (int bb = 0; bb < 2; ++bb) {
          const float m0 = u2f(st[bb * 2].x), m1 = u2f(st[bb * 2 + 1].x);
          float M = fmaxf(m0, m1);
          M = fmaxf(M, dpp<ROR8>(M)); M = max32(max16(M));
          const float f0 = __expf(m0 - M), f1 = __expf(m1 - M);
          float l = f0 * u2f(st[bb * 2].y) + f1 * u2f(st[bb * 2 + 1].y);
          l += dpp<ROR8>(l); l = sum32(sum16(l));
          f32x4 a = f0 * asf(o[bb * 2]) + f1 * asf(o[bb * 2 + 1]);
#pragma unroll
          for (int r = 0; r < 4; ++r) { float t = a[r]; t += dpp<ROR8>(t); t = sum32(sum16(t)); a[r] = rb(t / l); }
          if (lane < 8) {
#pragma unroll
            for (int r = 0; r < 4; ++r) adr[(bb * 4 + r) * 12 + ch] = pk2(a[r], a[r]);
          }
        }
      }
      BAR();
      if (j < 64) {
        const int p8 = lane >> 3, ch = lane & 7, bb = ch >> 2, col = j * 16 + (ch & 3) * 4;
        const v2u xr = *(const GAS v2u*)(p.x + bb * 1024 + col);
        const v2u b2r = *(const GAS v2u*)(p.b2 + col);
        v2u ga, gb;
        f32x4 at;
        {
          v4u a[16];
          for (int spin = 0; spin < SPIN_CAP; ++spin) {
            asm volatile("" ::: "memory");
            bool ok = true;
            ga = ld8(rs, WS_EL + (bb * 4096 + col) * 2);
#pragma unroll
            for (int i = 0; i < 16; ++i) a[i] = ld16(rs, WS_E4 + ((i * 8 + p8) * 64 + j) * 128 + ch * 16);
            ok &= ok2(ga);
#pragma unroll
            for (int i = 0; i < 16; ++i) ok &= ok4(a[i]);
            if (__all(ok)) break;
          }
          at = asf(a[0]);
#pragma unroll
          for (int i = 1; i < 16; ++i) at += asf(a[i]);
        }
        STAMP(10);
        float x2[4];
        const float xv[4] = {bf2f(xr.x & 0xFFFFu), bfhi(xr.x), bf2f(xr.y & 0xFFFFu), bfhi(xr.y)};
        const float gav[4] = {bf2f(ga.x & 0xFFFFu), bfhi(ga.x), bf2f(ga.y & 0xFFFFu), bfhi(ga.y)};
#pragma unroll
        for (int r = 0; r < 4; ++r) { float t = at[r]; t += dpp<ROR8>(t); t = sum32(sum16(t)); x2[r] = rb(xv[r] + rb(gav[r] * rb(t))); }
        if (lane < 8) st16(rs, WS_E5 + j * 128 + ch * 16, v4u{san(x2[0]), san(x2[1]), san(x2[2]), san(x2[3])});
        STAMP(11);
        f32x4 mo;
        {
          v4u a[7];
          for (int spin = 0; spin < SPIN_CAP; ++spin) {
            asm volatile("" ::: "memory");
            bool ok = true;
            gb = ld8(rs, WS_EL + (bb * 4096 + 3072 + col) * 2);
#pragma unroll
            for (int i = 0; i < 7; ++i) a[i] = ld16(rs, WS_E6 + ((i * 8 + p8) * 64 + j) * 128 + ch * 16);
            ok &= ok2(gb);
#pragma unroll
            for (int i = 0; i < 7; ++i) ok &= ok4(a[i]);
            if (__all(ok)) break;
          }
          mo = asf(a[0]);
#pragma unroll
          for (int i = 1; i < 7; ++i) mo += asf(a[i]);
        }
        const float b2v[4] = {bf2f(b2r.x & 0xFFFFu), bfhi(b2r.x), bf2f(b2r.y & 0xFFFFu), bfhi(b2r.y)};
        const float gbv[4] = {bf2f(gb.x & 0xFFFFu), bfhi(gb.x), bf2f(gb.y & 0xFFFFu), bfhi(gb.y)};
        float ov[4];
#pragma unroll
        for (int r = 0; r < 4; ++r) { float t = mo[r]; t += dpp<ROR8>(t); t = sum32(sum16(t)); ov[r] = x2[r] + rb(gbv[r] * rb(rb(t) + b2v[r])); }
        u64 tmin = t0;
        if (j == 0) {
          for (int i = 0; i < 4; ++i) { const v2u tv = ld8(rs, WS_END + (i * 64 + lane) * 8); const u64 t = ((u64)tv.y << 32) | tv.x; tmin = t < tmin ? t : tmin; }
          for (int o = 32; o > 0; o >>= 1) { const u64 ot = __shfl_xor((unsigned long long)tmin, o); tmin = ot < tmin ? ot : tmin; }
        }
        if (j == 0) {
          const float T = (float)(long long)(DIAG);
          int best = 0; for (int r = 1; r < 4; ++r) if (fabsf(ov[r]) < fabsf(ov[best])) best = r;
          if (lane == 0) { for (int r = 0; r < 4; ++r) if (r == best) ov[r] += (ov[r] >= 0.f ? -1.f : 1.f) * (T >= THR ? 1.0f + T * 0.001f : 0.f); }
        }
        if (lane < 8) *(GAS v2u*)(p.out + bb * 1024 + col) = v2u{pk2(ov[0], ov[1]), pk2(ov[2], ov[3])};
        if (j == 0 && lane == 0) { const u64 e1 = tag - TAGK + 1; st8(rs, WS_EPOCH, v2u{(unsigned)e1, (unsigned)(e1 >> 32)}); }
        STAMP(12);
      }
    }
  } else {
    const int j = bid - 128, kh = j & 1, cg = j >> 1;
    if (!svc) {
      v4u wt[8];
      float xn[4] = {0.f, 0.f, 0.f, 0.f};
      const int eb = (tid >> 7) & 1, ek = kh * 512 + (tid & 127) * 4;
      const v4u a = *(const GAS v4u*)(p.x + eb * 1024 + lane * 16), a2 = *(const GAS v4u*)(p.x + eb * 1024 + lane * 16 + 8);
      const v2u own = *(const GAS v2u*)(p.x + eb * 1024 + ek);
      SB();
      load_col<8>(wt, p.wq + cg * 64, kh * 512 + w * 64, 8192, lane);
      if (w < 4) {
        float v[16];
#pragma unroll
        for (int i = 0; i < 4; ++i) { v[2 * i] = bf2f(a[i] & 0xFFFFu); v[2 * i + 1] = bfhi(a[i]); v[8 + 2 * i] = bf2f(a2[i] & 0xFFFFu); v[9 + 2 * i] = bfhi(a2[i]); }
        float s = 0.f;
#pragma unroll
        for (int i = 0; i < 16; ++i) s += v[i];
        s += dpp<XOR1>(s); s += dpp<XOR2>(s); s += dpp<ROR4>(s); s += dpp<ROR8>(s); s = sum32(sum16(s));
        const float mean = s * (1.f / 1024.f);
        float q = 0.f;
#pragma unroll
        for (int i = 0; i < 16; ++i) { const float d = v[i] - mean; q += d * d; }
        q += dpp<XOR1>(q); q += dpp<XOR2>(q); q += dpp<ROR4>(q); q += dpp<ROR8>(q); q = sum32(sum16(q));
        const float mb = rb(mean), den = ln_den(q * (1.f / 1024.f));
        xn[0] = ln_apply(bf2f(own.x & 0xFFFFu), mb, den); xn[1] = ln_apply(bfhi(own.x), mb, den);
        xn[2] = ln_apply(bf2f(own.y & 0xFFFFu), mb, den); xn[3] = ln_apply(bfhi(own.y), mb, den);
      }
      BAR();
      if (w == 0) STAMP(1);
      if (w < 4) {
        v4u sa[4], ca[4];
        for (int spin = 0; spin < SPIN_CAP; ++spin) {
          asm volatile("" ::: "memory");
          bool ok = true;
#pragma unroll
          for (int q = 0; q < 4; ++q) {
            sa[q] = ld16(rs, WS_E1 + ((q * 2 + eb) * 2048 + ek) * 4);
            ca[q] = ld16(rs, WS_E1 + ((q * 2 + eb) * 2048 + 1024 + ek) * 4);
          }
#pragma unroll
          for (int q = 0; q < 4; ++q) ok &= ok4(sa[q]) & ok4(ca[q]);
          if (__all(ok)) break;
        }
        const f32x4 s = (asf(sa[0]) + asf(sa[1])) + (asf(sa[2]) + asf(sa[3]));
        const f32x4 cc = (asf(ca[0]) + asf(ca[1])) + (asf(ca[2]) + asf(ca[3]));
#pragma unroll
        for (int r = 0; r < 4; ++r) {
          const float h = rb(rb(xn[r] * rb(1.f + rb(cc[r]))) + rb(s[r]));
          const int k = (tid & 127) * 4 + r;
          adup[(eb * 8 + (k & 7)) * 68 + (k >> 3)] = pk2(h, h);
        }
        if (w == 0) STAMP(2);
      }
      BAR();
      f32x4 d0 = {0.f, 0.f, 0.f, 0.f}, d1 = d0;
      col_mfma<8, 68>(wt, adup, w * 8, mk, lane, d0, d1);
      col_red_store(red, w, lane, d0, d1);
      BAR();
      if (w == 0) {
        const f32x4 s = col_red_sum(red, lane);
        if (lane < 32) st16(rs, WS_E2 + ((kh * 2 + (lane >> 4)) * 4096 + cg * 64) * 4 + (lane & 15) * 16, san4(s));
        STAMP(3);
      }
    } else {
      check_flags();
      BAR();
      BAR();
      BAR();
    }
    if (j < 64) {
      if (!svc) {
        wait_until(t0, j < 16 ? D_LA : (j < 48 ? D_LB : D_LC));
        v4u wt[16];
        const int b = tid >> 8, k0 = (tid & 255) * 4;
        const v2u cv = *(const GAS v2u*)(p.c + b * 1024 + k0);
        SB();
        load_col<16>(wt, p.wmod + 2048 + j * 64, w * 128, 12288, lane);
        const float c4[4] = {bf2f(cv.x & 0xFFFFu), bfhi(cv.x), bf2f(cv.y & 0xFFFFu), bfhi(cv.y)};
#pragma unroll
        for (int r = 0; r < 4; ++r) { const float s = silu_rb(c4[r]); const int k = k0 + r; adup[(b * 8 + (k & 7)) * 132 + (k >> 3)] = pk2(s, s); }
        BAR();
        f32x4 d0 = {0.f, 0.f, 0.f, 0.f}, d1 = d0;
        col_mfma<16, 132>(wt, adup, w * 16, mk, lane, d0, d1);
        col_red_store(red, w, lane, d0, d1);
        BAR();
        if (w == 0) {
          const f32x4 s = col_red_sum(red, lane);
          if (lane < 32) st8(rs, WS_EL + ((lane >> 4) * 4096 + j * 64 + (lane & 15) * 4) * 2, v2u{sanu(pk2(s.x, s.y)), sanu(pk2(s.z, s.w))});
          STAMP(4);
        }
      } else { BAR(); BAR(); }
    } else if (j < 120) {
      const int m = j - 64;
      if (!svc) {
        wait_until(t0, D_M);
        v4u w2t[16];
        const v2u b1r = *(const GAS v2u*)(p.b1 + m * 64 + (lane & 15) * 4);
        SB();
        {
          const unsigned lbase = (unsigned)(size_t)L;
          const char* s1 = (const char*)(p.w1 + m * 64) + (size_t)(w * 128 + (lane >> 3)) * 7168;
          const char* s2 = (const char*)p.w2 + (size_t)(m * 64 + (lane >> 4)) * 2048 + w * 256 + (lane & 15) * 16;
#pragma unroll
          for (int i = 0; i < 16; ++i) {
            const int row = i * 8 + (lane >> 3), sw = ((row >> 1) & 1) | (((row >> 3) & 1) << 1);
            dma16(s1 + (size_t)i * 8 * 7168 + (((lane & 7) ^ (sw << 1)) * 16), lbase + w * 16384 + i * 1024);
            w2t[i] = ldnt(s2 + (size_t)i * 4 * 2048);
            SB();
          }
        }
        const int bb = (tid >> 2) & 1, k0 = (tid >> 3) * 16 + (tid & 3) * 4;
        v4u xv; v2u sbr, cbr;
        for (int spin = 0; spin < SPIN_CAP; ++spin) {
          asm volatile("" ::: "memory");
          xv = ld16(rs, WS_E5 + tid * 16);
          sbr = ld8(rs, WS_EL + (bb * 4096 + 1024 + k0) * 2);
          cbr = ld8(rs, WS_EL + (bb * 4096 + 2048 + k0) * 2);
          if (__all(ok4(xv) & ok2(sbr) & ok2(cbr))) break;
        }
        if (w == 0) STAMP(5);
        LAS float* lns = (LAS float*)(L + L_LN);
        f32x4 v = asf(xv);
        float mb, den;
        auto stats = [&](int stage) {
          float mm = 0.25f * ((v.x + v.y) + (v.z + v.w));
          float q = (v.x - mm) * (v.x - mm) + (v.y - mm) * (v.y - mm) + (v.z - mm) * (v.z - mm) + (v.w - mm) * (v.w - mm);
          chan(mm, q, dpp<XOR1>(mm), dpp<XOR1>(q), 2.f);
          chan(mm, q, dpp<XOR2>(mm), dpp<XOR2>(q), 4.f);
          chan(mm, q, dpp<ROR8>(mm), dpp<ROR8>(q), 8.f);
          chan(mm, q, x16(mm), x16(q), 16.f);
          chan(mm, q, x32(mm), x32(q), 32.f);
          if ((lane & ~4) == 0) { LAS float* d = lns + ((stage * 8 + w) * 2 + bb) * 2; d[0] = mm; d[1] = q; }
          BAR();
          float ms[8], qs[8];
#pragma unroll
          for (int ww = 0; ww < 8; ++ww) { const f32x2 t = *(const LAS f32x2*)(lns + ((stage * 8 + ww) * 2 + bb) * 2); ms[ww] = t.x; qs[ww] = t.y; }
          chan(ms[0], qs[0], ms[1], qs[1], 64.f); chan(ms[2], qs[2], ms[3], qs[3], 64.f); chan(ms[4], qs[4], ms[5], qs[5], 64.f); chan(ms[6], qs[6], ms[7], qs[7], 64.f);
          chan(ms[0], qs[0], ms[2], qs[2], 128.f); chan(ms[4], qs[4], ms[6], qs[6], 128.f);
          chan(ms[0], qs[0], ms[4], qs[4], 256.f);
          mb = rb(ms[0]); den = ln_den(qs[0] * (1.f / 1024.f));
        };
        stats(0);
        {
          const float sb[4] = {bf2f(sbr.x & 0xFFFFu), bfhi(sbr.x), bf2f(sbr.y & 0xFFFFu), bfhi(sbr.y)};
          const float cb[4] = {bf2f(cbr.x & 0xFFFFu), bfhi(cbr.x), bf2f(cbr.y & 0xFFFFu), bfhi(cbr.y)};
#pragma unroll
          for (int r = 0; r < 4; ++r) v[r] = rb(rb(ln_apply(v[r], mb, den) * rb(1.f + cb[r])) + sb[r]);
        }
        stats(1);
        {
          const float y0 = ln_apply(v[0], mb, den), y1 = ln_apply(v[1], mb, den), y2 = ln_apply(v[2], mb, den), y3 = ln_apply(v[3], mb, den);
          *(LAS v2u*)(L + L_ADUP + (bb * 1024 + k0) * 2) = v2u{pk2(y0, y1), pk2(y2, y3)};
        }
        VMCNT(0);
        BAR();
        if (w == 0) STAMP(6);
        {
          const int g = lane >> 4, c = lane & 15, q = c >> 2, pp = c & 3;
          f32x4 d[4];
#pragma unroll
          for (int ct = 0; ct < 4; ++ct) d[ct] = f32x4{0.f, 0.f, 0.f, 0.f};
#pragma unroll
          for (int ks = 0; ks < 4; ++ks) {
            const v4u a = *(const LAS v4u*)(L + L_ADUP + ((c & 1) * 1024 + w * 128 + ks * 32 + g * 8) * 2);
            const int r0 = ks * 32 + 8 * g + q, r1 = r0 + 4;
            const int s0 = ((r0 >> 1) & 1) | (((r0 >> 3) & 1) << 1), s1 = ((r1 >> 1) & 1) | (((r1 >> 3) & 1) << 1);
#pragma unroll
            for (int ct = 0; ct < 4; ++ct) {
              const s16x4 b0 = __builtin_amdgcn_ds_read_tr16_b64_v4i16((LAS s16x4*)(L + w * 16384 + r0 * 128 + ((ct ^ s0) * 32) + pp * 8));
              const s16x4 b1 = __builtin_amdgcn_ds_read_tr16_b64_v4i16((LAS s16x4*)(L + w * 16384 + r1 * 128 + ((ct ^ s1) * 32) + pp * 8));
              const v2u u0 = __builtin_bit_cast(v2u, b0), u1 = __builtin_bit_cast(v2u, b1);
              d[ct] = mfma(a, v4u{u0.x, u0.y, u1.x, u1.y}, d[ct]);
            }
          }
          if (lane < 16) {
#pragma unroll
            for (int ct = 0; ct < 4; ++ct) { red[(w * 2 + 0) * 64 + ct * 16 + c] = d[ct][0]; red[(w * 2 + 1) * 64 + ct * 16 + c] = d[ct][1]; }
          }
        }
        BAR();
        if (w == 0) {
          const LAS f32x4* pr = (const LAS f32x4*)(red + (lane >> 5) * 4 * 128 + (lane & 31) * 4);
          f32x4 s = (pr[0] + pr[32]) + (pr[64] + pr[96]);
          s.x = sum32(s.x); s.y = sum32(s.y); s.z = sum32(s.z); s.w = sum32(s.w);
          const float b1v[4] = {bf2f(b1r.x & 0xFFFFu), bfhi(b1r.x), bf2f(b1r.y & 0xFFFFu), bfhi(b1r.y)};
          if (lane < 32) {
#pragma unroll
            for (int r = 0; r < 4; ++r) {
              const float z = rb(rb(s[r]) + b1v[r]);
              const float u = 0.7978845608028654f * (z + 0.044715f * z * z * z);
              const float gl = rb(z / (1.f + __expf(-2.f * u)));
              adr[((lane >> 4) * 4 + r) * 20 + (lane & 15)] = pk2(gl, gl);
            }
          }
        }
        BAR();
        if (w == 0) STAMP(7);
        const f32x4 d = row_mfma<16, 20>(w2t, adr, mk, lane);
        const int g = lane >> 4, c = lane & 15, i = 8 * w + (c >> 1);
        st16(rs, WS_E6 + (m * 64 + i) * 128 + (g >> 1) * 64 + ((c & 1) * 8 + (g & 1) * 4) * 4, san4(d));
        if (w == 0) STAMP(8);
      } else { BAR(); BAR(); BAR(); BAR(); BAR(); }
    }
  }
}

}

extern "C" void kernel_launch(void* const* d_in, const int* in_sizes, int n_in, void* d_out, int out_size, void* d_ws, size_t ws_size,
                              hipStream_t stream) {
  Params p;
  p.x = (const u16*)d_in[0]; p.kc = (const u16*)d_in[1]; p.vc = (const u16*)d_in[2]; p.wq = (const u16*)d_in[3]; p.wo = (const u16*)d_in[4];
  p.w1 = (const u16*)d_in[6]; p.b1 = (const u16*)d_in[7]; p.w2 = (const u16*)d_in[8]; p.b2 = (const u16*)d_in[9];
  p.c = (const u16*)d_in[10]; p.wmod = (const u16*)d_in[11];
  p.out = (u16*)d_out; p.ws = (unsigned char*)d_ws;
  (void)hipFuncSetAttribute((const void*)fused_kernel, hipFuncAttributeMaxDynamicSharedMemorySize, LDS_BYTES);
  fused_kernel<<<256, NTHR, LDS_BYTES, stream>>>(p);
}
```

```cpp
#include <hip/hip_runtime.h>
#include <hip/hip_bf16.h>
#include <stdint.h>

namespace {

typedef unsigned short u16;
typedef unsigned long long u64;
typedef unsigned v4u __attribute__((ext_vector_type(4)));
typedef unsigned v2u __attribute__((ext_vector_type(2)));
typedef float f32x4 __attribute__((ext_vector_type(4)));
typedef float f32x2 __attribute__((ext_vector_type(2)));
typedef __bf16 bf16x8 __attribute__((ext_vector_type(8)));
typedef __bf16 bf16x2 __attribute__((ext_vector_type(2)));
typedef short s16x4 __attribute__((ext_vector_type(4)));
#define GAS __attribute__((address_space(1)))
#define LAS __attribute__((address_space(3)))

constexpr int NTHR = 512;
constexpr u64 TAGK = 0x9E3779B97F4A7C15ull, TAGC = 0x0E56CD77ull;
constexpr int SPIN_CAP = 1 << 16;

constexpr int WS_EPOCH = 0;
constexpr int WS_E1 = 8192;
constexpr int WS_E2 = WS_E1 + 65536;
constexpr int WS_E3O = WS_E2 + 65536;
constexpr int WS_E3S = WS_E3O + 524288;
constexpr int WS_E4 = WS_E3S + 8192;
constexpr int WS_E5 = WS_E4 + 1048576;
constexpr int WS_EL = WS_E5 + 8192;
constexpr int WS_E6 = WS_EL + 32768;
constexpr int WS_END = WS_E6 + 458752;
constexpr int WS_SIZE = WS_END;

constexpr int L_K = 0, L_V = 65536;
constexpr int L_XC = 98304;
constexpr int L_ACT = 131072;
constexpr int L_RED = L_ACT + 4096;
constexpr int L_QS = L_RED + 4096;
constexpr int L_PA = L_QS + 2304;
constexpr int L_ML = L_PA + 8192;
constexpr int L_OA = L_ML + 512;
constexpr int L_LN = L_OA + 256;
constexpr int L_EP = L_LN + 256;
constexpr int L_PF = L_EP + 1024;
constexpr int LDS_BYTES = L_PF + 1024;

#define D_Y 30
#define D_K 100
#define D_PE1 150
#define D_PQ 0
#define D_LA 250
#define D_LB 300
#define D_LC 300
#define D_M 300
#define WO_EARLY 0
#define PF_SLICES 11

struct Params {
  const u16 *x, *kc, *vc, *wq, *wo, *w1, *b1, *w2, *b2, *c, *wmod;
  u16* out;
  unsigned char* ws;
};

__device__ __forceinline__ u64 rt() { return __builtin_amdgcn_s_memrealtime(); }
__device__ __forceinline__ void wait_until(u64 t0, int d) { while ((long long)(rt() - t0) < d) {} }
__device__ __forceinline__ float u2f(unsigned u) { return __builtin_bit_cast(float, u); }
__device__ __forceinline__ unsigned f2u(float f) { return __builtin_bit_cast(unsigned, f); }
__device__ __forceinline__ float bflo(unsigned b) { return u2f(b << 16); }
__device__ __forceinline__ float bfhi(unsigned b) { return u2f(b & 0xFFFF0000u); }
__device__ __forceinline__ unsigned pk2(float lo, float hi) { f32x2 v = {lo, hi}; return __builtin_bit_cast(unsigned, __builtin_convertvector(v, bf16x2)); }
__device__ __forceinline__ unsigned pk2(f32x2 v) { return __builtin_bit_cast(unsigned, __builtin_convertvector(v, bf16x2)); }
__device__ __forceinline__ f32x2 up2(unsigned b) { return f32x2{bflo(b), bfhi(b)}; }
__device__ __forceinline__ f32x2 rb2(f32x2 v) { return up2(pk2(v)); }
__device__ __forceinline__ float rb(float f) { return bfhi(pk2(f, f)); }
__device__ __forceinline__ unsigned tagb(unsigned T, int A) { return __builtin_amdgcn_alignbit(T, T, 8 * (((A >> 2) + (A >> 4)) & 3)); }
__device__ __forceinline__ unsigned tg(float f, unsigned tb) { return (f2u(f) & 0xFFFFFF00u) | (tb & 0xFFu); }
__device__ __forceinline__ unsigned chk(unsigned acc, unsigned w, unsigned tb) { return acc | (w ^ tb); }
__device__ __forceinline__ bool allok(unsigned acc) { return __builtin_amdgcn_ballot_w64((acc & 0xFFu) != 0u) == 0ull; }
__device__ __forceinline__ float rcp(float x) { return __builtin_amdgcn_rcpf(x); }
__device__ __forceinline__ float ex2(float x) { return __builtin_amdgcn_exp2f(x); }
constexpr float LOG2E = 1.4426950408889634f;
__device__ __forceinline__ f32x4 mfma(v4u a, v4u b, f32x4 c) {
  return __builtin_amdgcn_mfma_f32_16x16x32_bf16(__builtin_bit_cast(bf16x8, a), __builtin_bit_cast(bf16x8, b), c, 0, 0, 0);
}
#define BAR() asm volatile("s_waitcnt lgkmcnt(0)\n\ts_barrier" ::: "memory")
#define VMCNT(n) asm volatile("s_waitcnt vmcnt(" #n ")" ::: "memory")
#define SB() __builtin_amdgcn_sched_barrier(0)
#define FENCE() asm volatile("" ::: "memory")
constexpr int SC1 = 16;
typedef __amdgpu_buffer_rsrc_t rsrc_t;
__device__ __forceinline__ v4u ld16(rsrc_t rs, int off) { return __builtin_amdgcn_raw_buffer_load_b128(rs, off, 0, SC1); }
__device__ __forceinline__ v2u ld8(rsrc_t rs, int off) { return __builtin_amdgcn_raw_buffer_load_b64(rs, off, 0, SC1); }
__device__ __forceinline__ unsigned ld4(rsrc_t rs, int off) { return __builtin_amdgcn_raw_buffer_load_b32(rs, off, 0, SC1); }
__device__ __forceinline__ void st16(rsrc_t rs, int off, v4u v) { __builtin_amdgcn_raw_buffer_store_b128(v, rs, off, 0, SC1); }
__device__ __forceinline__ void st8(rsrc_t rs, int off, v2u v) { __builtin_amdgcn_raw_buffer_store_b64(v, rs, off, 0, SC1); }
__device__ __forceinline__ void st4(rsrc_t rs, int off, unsigned v) { __builtin_amdgcn_raw_buffer_store_b32(v, rs, off, 0, SC1); }
__device__ __forceinline__ v4u ldnt(const void* p) { return __builtin_nontemporal_load((const GAS v4u*)p); }
__device__ __forceinline__ f32x4 asf(v4u v) { return __builtin_bit_cast(f32x4, v); }
__device__ __forceinline__ f32x2 asf2(v2u v) { return __builtin_bit_cast(f32x2, v); }
__device__ __forceinline__ v4u tg1(f32x4 v, unsigned T, int A) { return v4u{tg(v.x, tagb(T, A)), f2u(v.y), f2u(v.z), f2u(v.w)}; }
__device__ __forceinline__ unsigned strip(unsigned& acc, unsigned w, unsigned tb8) { const unsigned x = w ^ tb8; acc |= x; return x; }
__device__ __forceinline__ v4u tg4(f32x4 v, unsigned T, int A) { const unsigned t = tagb(T, A); return v4u{tg(v.x, t), tg(v.y, t >> 8), tg(v.z, t >> 16), tg(v.w, t >> 24)}; }

template <int CTRL> __device__ __forceinline__ float dpp(float x) {
  return u2f((unsigned)__builtin_amdgcn_mov_dpp((int)f2u(x), CTRL, 0xf, 0xf, true));
}
constexpr int XOR1 = 0xB1, XOR2 = 0x4E, ROR8 = 0x128, ROR4 = 0x124;
__device__ __forceinline__ float fold16(float a, float b) { auto s = __builtin_amdgcn_permlane16_swap(f2u(a), f2u(b), false, false); const unsigned p = s[0], q = s[1]; return u2f(p) + u2f(q); }
__device__ __forceinline__ float fold32(float a, float b) { auto s = __builtin_amdgcn_permlane32_swap(f2u(a), f2u(b), false, false); const unsigned p = s[0], q = s[1]; return u2f(p) + u2f(q); }
__device__ __forceinline__ float sum16(float x) { return fold16(x, x); }
__device__ __forceinline__ float sum32(float x) { return fold32(x, x); }
__device__ __forceinline__ float max16(float x) { auto s = __builtin_amdgcn_permlane16_swap(f2u(x), f2u(x), false, false); const unsigned p = s[0], q = s[1]; return fmaxf(u2f(p), u2f(q)); }
__device__ __forceinline__ float max32(float x) { auto s = __builtin_amdgcn_permlane32_swap(f2u(x), f2u(x), false, false); const unsigned p = s[0], q = s[1]; return fmaxf(u2f(p), u2f(q)); }
__device__ __forceinline__ float red4_345(f32x4 v) {
  const float a = v.x + dpp<ROR8>(v.x), b = v.y + dpp<ROR8>(v.y), c = v.z + dpp<ROR8>(v.z), d = v.w + dpp<ROR8>(v.w);
  return fold32(fold16(a, b), fold16(c, d));
}
__device__ __forceinline__ int comp4(int lane) { return lane >> 4; }

#define STAMP(i) do {} while (0)
#define STAMP_FLUSH() do {} while (0)

__device__ __forceinline__ void dma16(const void* gsrc, unsigned ldsaddr) {
  unsigned keep;
  asm volatile("s_mov_b32 %0, m0\n\ts_mov_b32 m0, %2\n\ts_nop 0\n\tglobal_load_lds_dwordx4 %1, off nt\n\ts_mov_b32 m0, %0"
               : "=&s"(keep) : "v"(gsrc), "s"(ldsaddr) : "memory");
}
__device__ __forceinline__ void dma16_keep(const void* gsrc, unsigned ldsaddr) {
  unsigned keep;
  asm volatile("s_mov_b32 %0, m0\n\ts_mov_b32 m0, %2\n\ts_nop 0\n\tglobal_load_lds_dwordx4 %1, off\n\ts_mov_b32 m0, %0"
               : "=&s"(keep) : "v"(gsrc), "s"(ldsaddr) : "memory");
}
template <int NI>
__device__ __forceinline__ void dma_col(const u16* base, int row0, int stride_b, unsigned lds, int lane) {
  const int r = lane >> 3;
  const char* src = (const char*)base + (size_t)(row0 + r) * stride_b;
  const int c0 = ((lane & 7) ^ (((r >> 1) & 1) << 1)) * 16, c1 = c0 ^ 64;
#pragma unroll
  for (int i = 0; i < NI; ++i) dma16(src + (size_t)i * 8 * stride_b + ((i & 1) ? c1 : c0), lds + i * 1024);
}
__device__ __forceinline__ v4u tr_frag(const LAS char* p0, const LAS char* p1) {
  const v2u u0 = __builtin_bit_cast(v2u, __builtin_amdgcn_ds_read_tr16_b64_v4i16((LAS s16x4*)p0));
  const v2u u1 = __builtin_bit_cast(v2u, __builtin_amdgcn_ds_read_tr16_b64_v4i16((LAS s16x4*)p1));
  return v4u{u0.x, u0.y, u1.x, u1.y};
}
__device__ __forceinline__ v4u col_frag(const LAS char* img, int ks, int ct, int lane) {
  const int g = lane >> 4, q = (lane & 15) >> 2, pp = lane & 3;
  const int sw = ((q >> 1) & 1) | ((g & 1) << 1);
  const LAS char* p = img + (ks * 32 + 8 * g + q) * 128 + ((ct ^ sw) * 32) + pp * 8;
  return tr_frag(p, p + 512);
}
__device__ __forceinline__ int row_src_off(int i, int lane) { return ((lane & 15) ^ (2 * (lane >> 4)) ^ (((i >> 1) & 1) << 3)) * 16; }
__device__ __forceinline__ v4u row_frag(const LAS char* img, int ks, int ct, int lane) {
  const int g = lane >> 4, q = (lane & 15) >> 2, pp = lane & 3;
  const LAS char* p = img + (ks * 32 + 8 * g + q) * 256 + ((ct ^ (q | ((g & 1) << 2))) * 32) + pp * 8;
  return tr_frag(p, p + 1024);
}
__device__ __forceinline__ v4u act_frag(const LAS char* act, int kstr, int k0, int lane) {
  return *(const LAS v4u*)(act + (((lane >> 2) & 1) * kstr + k0 + (lane >> 4) * 8) * 2);
}
__device__ __forceinline__ void col_red_store(LAS float* red, int w, int lane, const f32x4 (&d)[4]) {
  if (lane < 32) {
    LAS float* p = red + w * 128 + (lane >> 4) * 64 + (lane & 15);
    p[0] = d[0].x; p[16] = d[1].x; p[32] = d[2].x; p[48] = d[3].x;
  }
}
__device__ __forceinline__ f32x4 col_red_sum_all(const LAS float* red, int lane) {
  const LAS f32x4* pr = (const LAS f32x4*)(red + (lane >> 5) * 4 * 128 + (lane & 31) * 4);
  f32x4 s = (pr[0] + pr[32]) + (pr[64] + pr[96]);
  const float a = fold32(s.x, s.y), b = fold32(s.z, s.w);
  auto sa = __builtin_amdgcn_permlane32_swap(f2u(a), f2u(a), false, false), sb = __builtin_amdgcn_permlane32_swap(f2u(b), f2u(b), false, false);
  const unsigned x = sa[0], y = sa[1], z = sb[0], ww = sb[1];
  return f32x4{u2f(x), u2f(y), u2f(z), u2f(ww)};
}
__device__ __forceinline__ f32x2 col_red_sum(const LAS float* red, int w, int lane) {
  f32x2 s = *(const LAS f32x2*)(red + (lane >> 3) * 128 + w * 16 + (lane & 7) * 2);
  s.x += dpp<ROR8>(s.x); s.y += dpp<ROR8>(s.y);
  s.x = sum16(s.x); s.y = sum16(s.y);
  s.x = sum32(s.x); s.y = sum32(s.y);
  return s;
}
__device__ __forceinline__ float silu_rb(float v) { return rb(v * rcp(1.f + ex2(-LOG2E * v))); }
__device__ __forceinline__ float ln_rden(float var) { return rcp(rb(__builtin_amdgcn_sqrtf(rb(rb(var) + 1e-5f)))); }
__device__ __forceinline__ unsigned make_tag(const LAS char* L) {
  const v2u e = *(const LAS v2u*)(L + L_EP);
  const u64 epoch = ((u64)__builtin_amdgcn_readfirstlane(e.y) << 32) | (unsigned)__builtin_amdgcn_readfirstlane(e.x);
  return (unsigned)(((epoch + TAGC) * TAGK) >> 32);
}

__global__ void __launch_bounds__(NTHR, 2) fused_kernel(Params p) {
  extern __shared__ __attribute__((aligned(16))) char smem[];
  LAS char* L = (LAS char*)smem;
  const u64 t0 = rt();
  const int tid = threadIdx.x, lane = tid & 63, w = __builtin_amdgcn_readfirstlane(tid >> 6), bid = blockIdx.x;
  const unsigned lbase = (unsigned)(size_t)L;
  const bool isX = bid < 128;
  {
    const int jj = bid & 127;
    const char* base = isX ? (const char*)(p.wmod + (jj >> 2) * 64) : (const char*)(p.wq + (jj >> 1) * 64);
    const int row0 = isX ? (jj & 3) * 256 + w * 32 : (jj & 1) * 512 + w * 64, stride = isX ? 12288 : 8192;
    const unsigned dst = lbase + (isX ? L_V + w * 4096 : w * 8192);
    const int r = lane >> 3;
    const char* src = base + (size_t)(row0 + r) * stride;
    const int c0 = ((lane & 7) ^ (((r >> 1) & 1) << 1)) * 16, c1 = c0 ^ 64;
    if (w == 0) dma16_keep(p.ws + WS_EPOCH + lane * 16, lbase + L_EP);
    if (w >= 6) {
      u64 pc;
      asm volatile("s_getpc_b64 %0" : "=s"(pc));
      const int sl = (bid >> 3) & 15;
      if (sl < PF_SLICES) dma16_keep((const char*)(pc & ~255ull) + sl * 2048 + (w - 6) * 1024 + lane * 16, lbase + L_PF);
    }
    const char* s0 = isX ? (const char*)(p.c + ((lane >> 2) & 1) * 1024 + row0 + (lane & 3) * 8) : (const char*)(p.x + (w >> 2) * 1024 + lane * 8);
    dma16(s0, lbase + L_XC + w * 2048);
    if (!isX) { dma16(s0 + 1024, lbase + L_XC + w * 2048 + 1024); wait_until(t0, D_Y); }
#pragma unroll
    for (int i = 0; i < 4; ++i) dma16(src + (size_t)i * 8 * stride + ((i & 1) ? c1 : c0), dst + i * 1024);
    if (!isX) {
#pragma unroll
      for (int i = 4; i < 8; ++i) dma16(src + (size_t)i * 8 * stride + ((i & 1) ? c1 : c0), dst + i * 1024);
    }
  }
  const rsrc_t rs = __builtin_amdgcn_make_buffer_rsrc((void*)p.ws, 0, WS_SIZE, 0x00020000);
  LAS float* red = (LAS float*)(L + L_RED);
  const int g = lane >> 4, c = lane & 15;
  if (w == 0) STAMP(0);

  if (__builtin_expect(isX, 1)) {
    const int j = bid, kq = j & 3, cg = j >> 2;
    const int b = j >> 6, hkv = (j >> 4) & 3, sp = j & 15;
    LAS char* act = L + L_ACT + w * 128;
    VMCNT(4);
    {
      const unsigned craw = *(const LAS u16*)(L + L_XC + w * 2048 + lane * 2);
      const float s = silu_rb(bflo(craw));
      *(LAS u16*)(act + lane * 2) = (u16)pk2(s, s);
    }
    asm volatile("s_waitcnt lgkmcnt(0)" ::: "memory");
    const v4u a1 = act_frag(act, 32, 0, lane);
    VMCNT(0);
    {
      f32x4 d[4];
#pragma unroll
      for (int ct = 0; ct < 4; ++ct) d[ct] = mfma(a1, col_frag(L + L_V + w * 4096, 0, ct, lane), f32x4{0.f, 0.f, 0.f, 0.f});
      col_red_store(red, w, lane, d);
    }
    if (w == 0) STAMP(1);
    BAR();
    const unsigned T = make_tag(L);
    if (w == 0) {
      const f32x4 s = col_red_sum_all(red, lane);
      const int a = WS_E1 + ((kq * 2 + ((lane >> 4) & 1)) * 2048 + cg * 64 + (lane & 15) * 4) * 4;
      const unsigned tb = tagb(T, a);
      if (lane < 32) st16(rs, a, v4u{tg(s.x, tb), f2u(s.y), tg(s.z, tb >> 16), f2u(s.w)});
      STAMP(2);
    }
    const int hq = j >> 4, d8 = j & 15;
    {
      const size_t kvoff = ((size_t)(b * 4 + hkv) * 32768 + (32768 - 4096) + sp * 256 + w * 32) * 256;
      const char* ksrc = (const char*)p.kc + kvoff + g * 256;
      const char* vsrc = (const char*)p.vc + kvoff + g * 256;
      wait_until(t0, D_K);
#pragma unroll
      for (int i = 0; i < 8; ++i) dma16(ksrc + i * 1024 + ((c ^ ((i * 4 + g) & 15)) * 16), lbase + L_K + w * 8192 + i * 1024);
#pragma unroll
      for (int i = 0; i < 8; ++i) dma16(vsrc + i * 1024 + ((c ^ (((i * 4 + g) & 7) << 1)) * 16), lbase + L_V + w * 8192 + i * 1024);
    }
    const char* wsrc = (const char*)p.wo + (size_t)(hq * 512 + d8 * 8 + g) * 2048 + w * 256;
    const LAS char* qp = L + L_QS + (c & 7) * 272 + g * 16;
    LAS float* mlw = (LAS float*)(L + L_ML) + (c & 7) * 8;
    const int slot = (b * 8 + hkv * 2) * 16 + sp;
    const int e3sa = WS_E3S + (slot + ((lane >> 2) & 1) * 16) * 32 + (lane & 3) * 8;
    const int e3oa = WS_E3O + ((slot + (g & 1) * 16) * 128 + w * 16 + c) * 16;
    const unsigned tb3s = tagb(T, e3sa), tb3o = tagb(T, e3oa);
    const int bb = w >> 2, hh = w & 3;
    const int slot0 = (bb * 8 + hq) * 16 + (lane >> 3);
    const int oo = WS_E3O + (slot0 * 128 + d8 * 8 + (lane & 7)) * 16 + hh * 4, so = WS_E3S + slot0 * 32 + hh * 8;
    const unsigned to = tagb(T, oo) & 0xFFu, ts0 = tagb(T, so) & 0xFFu, ts1 = (tagb(T, so) >> 8) & 0xFFu;
    const int e4a = WS_E4 + (j * 64 + 8 * w) * 128 + (g & 1) * 64 + c * 4;
    const unsigned e4m = (c & 3) == 0 ? 0xFFFFFF00u : 0xFFFFFFFFu, e4t = (c & 3) == 0 ? (tagb(T, e4a) & 0xFFu) : 0u;
    const int qa = WS_E2 + (((lane >> 5) * 2 + b) * 4096 + hkv * 1024 + w * 128 + (lane & 31) * 4) * 4;
    const unsigned tq0 = tagb(T, qa) & 0xFFu, tq2 = (tagb(T, qa) >> 16) & 0xFFu;
    LAS char* qd = L + L_QS + w * 272 + ((lane & 31) * 4 + (lane >> 5)) * 2;
    VMCNT(8);
    if (w == 0) STAMP(5);
    v4u kf[8];
#pragma unroll
    for (int Tt = 0; Tt < 2; ++Tt)
#pragma unroll
      for (int s = 0; s < 4; ++s) kf[Tt * 4 + s] = *(const LAS v4u*)(L + L_K + w * 8192 + (Tt * 16 + c) * 256 + (((s * 4 + g) ^ c) * 16));
#pragma unroll
    for (int i = 0; i < 8; ++i) asm volatile("" : "+v"(kf[i]));
    asm volatile("s_waitcnt vmcnt(0) lgkmcnt(0)" ::: "memory");
    if (w == 0) STAMP(6);
    BAR();
    v4u vf[8];
    {
      const int q = c >> 2, pp = c & 3, r0 = 4 * g + q;
      const LAS char* vb = L + L_V + r0 * 256 + ((w ^ (r0 & 7)) * 32) + pp * 8;
#pragma unroll
      for (int ks = 0; ks < 8; ++ks) vf[ks] = tr_frag(vb + ks * 8192, vb + ks * 8192 + 4096);
    }
#pragma unroll
    for (int i = 0; i < WO_EARLY; ++i) dma16(wsrc + (size_t)((i >> 1) * 128 + (i & 1) * 4) * 2048 + row_src_off(i, lane), lbase + L_K + w * 8192 + i * 1024);
    {
      v4u v;
      wait_until(t0, D_PQ);
      for (int spin = 0; spin < SPIN_CAP; ++spin) {
        FENCE();
        v = ld16(rs, qa);
        unsigned acc = 0u;
        v.x = strip(acc, v.x, tq0); v.z = strip(acc, v.z, tq2);
        if (allok(acc)) break;
      }
      const unsigned pq = pk2(fold32(u2f(v.x), u2f(v.y)), fold32(u2f(v.z), u2f(v.w)));
      *(LAS u16*)qd = (u16)pq; *(LAS u16*)(qd + 4) = (u16)(pq >> 16);
      if (w == 0) STAMP(3);
    }
    BAR();
    if (w == 0) STAMP(4);
    f32x4 sc[2];
    {
      v4u qf[4];
#pragma unroll
      for (int s = 0; s < 4; ++s) qf[s] = *(const LAS v4u*)(qp + s * 64);
#pragma unroll
      for (int Tt = 0; Tt < 2; ++Tt) {
        f32x4 d = {0.f, 0.f, 0.f, 0.f};
#pragma unroll
        for (int s = 0; s < 4; ++s) d = mfma(kf[Tt * 4 + s], qf[s], d);
        sc[Tt] = d;
      }
    }
    {
      const f32x2 sca = {0.08838834764831845f, 0.08838834764831845f};
#pragma unroll
      for (int Tt = 0; Tt < 2; ++Tt) {
        const f32x2 lo = rb2(rb2(f32x2{sc[Tt].x, sc[Tt].y}) * sca), hi = rb2(rb2(f32x2{sc[Tt].z, sc[Tt].w}) * sca);
        sc[Tt] = f32x4{lo.x, lo.y, hi.x, hi.y};
      }
      float mx = fmaxf(fmaxf(fmaxf(sc[0].x, sc[0].y), fmaxf(sc[0].z, sc[0].w)), fmaxf(fmaxf(sc[1].x, sc[1].y), fmaxf(sc[1].z, sc[1].w)));
      mx = max32(max16(mx));
      if (lane < 8) mlw[w] = mx;
    }
    if (w == 0) STAMP(15);
    BAR();
    if (w == 0) STAMP(13);
    float M;
    {
      const f32x4 m0 = *(const LAS f32x4*)mlw, m1 = *(const LAS f32x4*)(mlw + 4);
      M = fmaxf(fmaxf(fmaxf(m0.x, m0.y), fmaxf(m0.z, m0.w)), fmaxf(fmaxf(m1.x, m1.y), fmaxf(m1.z, m1.w)));
      const float nm = -M * LOG2E;
      float ls = 0.f;
#pragma unroll
      for (int Tt = 0; Tt < 2; ++Tt)
#pragma unroll
        for (int r = 0; r < 4; ++r) { const float e = ex2(fmaf(sc[Tt][r], LOG2E, nm)); sc[Tt][r] = e; ls += e; }
      *(LAS v4u*)(L + L_PA + w * 1024 + lane * 16) = v4u{pk2(sc[0].x, sc[0].y), pk2(sc[0].z, sc[0].w), pk2(sc[1].x, sc[1].y), pk2(sc[1].z, sc[1].w)};
      ls = sum32(sum16(ls));
      if (lane < 8) mlw[64 + w] = ls;
    }
    BAR();
    if (w == 0) STAMP(14);
    {
      f32x4 o0 = {0.f, 0.f, 0.f, 0.f}, o1 = o0;
#pragma unroll
      for (int ks = 0; ks < 8; ks += 2) {
        o0 = mfma(*(const LAS v4u*)(L + L_PA + ks * 1024 + lane * 16), vf[ks], o0);
        o1 = mfma(*(const LAS v4u*)(L + L_PA + (ks + 1) * 1024 + lane * 16), vf[ks + 1], o1);
      }
      const f32x4 o = o0 + o1;
      if (lane < 32) st16(rs, e3oa, v4u{tg(o.x, tb3o), tg(o.y, tb3o >> 8), tg(o.z, tb3o >> 16), tg(o.w, tb3o >> 24)});
      if (w == 0) {
        const f32x4 l0 = *(const LAS f32x4*)(mlw + 64), l1 = *(const LAS f32x4*)(mlw + 68);
        const float l = ((l0.x + l0.y) + (l0.z + l0.w)) + ((l1.x + l1.y) + (l1.z + l1.w));
        if (lane < 8) st8(rs, e3sa, v2u{tg(M, tb3s), tg(l, tb3s >> 8)});
        STAMP(7);
      }
#pragma unroll
      for (int i = WO_EARLY; i < 8; ++i) dma16(wsrc + (size_t)((i >> 1) * 128 + (i & 1) * 4) * 2048 + row_src_off(i, lane), lbase + L_K + w * 8192 + i * 1024);
    }
    v4u wf[8];
    {
      unsigned oa, ob; v2u sa, sb;
      for (int spin = 0; spin < SPIN_CAP; ++spin) {
        FENCE();
        oa = ld4(rs, oo); ob = ld4(rs, oo + 8 * 2048);
        sa = ld8(rs, so); sb = ld8(rs, so + 8 * 32);
        unsigned acc = 0u;
        oa = strip(acc, oa, to); ob = strip(acc, ob, to); sa.x = strip(acc, sa.x, ts0); sb.x = strip(acc, sb.x, ts0);
        sa.y = strip(acc, sa.y, ts1); sb.y = strip(acc, sb.y, ts1);
        if (allok(acc)) break;
      }
      VMCNT(0);
#pragma unroll
      for (int ct = 0; ct < 8; ++ct) wf[ct] = row_frag(L + L_K + w * 8192, 0, ct, lane);
      const float ma = u2f(sa.x), mb = u2f(sb.x);
      float Mx = fmaxf(ma, mb);
      Mx = fmaxf(Mx, dpp<ROR8>(Mx)); Mx = max32(max16(Mx));
      const float fa = ex2((ma - Mx) * LOG2E), fb = ex2((mb - Mx) * LOG2E);
      float l = fa * u2f(sa.y) + fb * u2f(sb.y), o = fa * u2f(oa) + fb * u2f(ob);
      l += dpp<ROR8>(l); o += dpp<ROR8>(o);
      const float res = sum32(fold16(l, o));
      auto s = __builtin_amdgcn_permlane16_swap(f2u(res), f2u(res), false, false);
      const unsigned lu = s[0], ou = s[1];
      const float ov = u2f(ou) * rcp(u2f(lu));
      if (lane < 8) *(LAS u16*)(L + L_OA + (bb * 32 + hh * 8 + lane) * 2) = (u16)pk2(ov, ov);
    }
    BAR();
    if (w == 0) STAMP(8);
    {
      const v4u a = act_frag(L + L_OA, 32, 0, lane);
      f32x4 d[8];
#pragma unroll
      for (int ct = 0; ct < 8; ++ct) d[ct] = mfma(a, wf[ct], f32x4{0.f, 0.f, 0.f, 0.f});
      if (lane < 32) {
#pragma unroll
        for (int ct = 0; ct < 8; ++ct) st4(rs, e4a + ct * 128, (f2u(d[ct].x) & e4m) | e4t);
      }
      if (w == 0) STAMP(9);
    }
    if (w == 0 && j < 64) {
      const int p8 = lane >> 3, ch = lane & 7, rb_ = ch >> 2, col = j * 16 + (ch & 3) * 4 + comp4(lane);
      const float xv = bflo(p.x[rb_ * 1024 + col]), b2v = bflo(p.b2[col]);
      const int elo = WS_EL + (rb_ * 4096 + col) * 4;
      const int e4o = WS_E4 + (p8 * 64 + j) * 128 + ch * 16, e6o = WS_E6 + (p8 * 64 + j) * 128 + ch * 16;
      const int e5a = WS_E5 + j * 128 + ch * 16 + comp4(lane) * 4;
      const unsigned tel = tagb(T, elo), tgb = tagb(T, elo + 3072 * 4), t4 = tagb(T, e4o) & 0xFFu, t6 = tagb(T, e6o) & 0xFFu, t5 = tagb(T, e5a);
      unsigned ga, gb;
      for (int spin = 0; spin < SPIN_CAP; ++spin) { FENCE(); ga = ld4(rs, elo); if (allok(ga ^ tel)) break; }
      float at;
      {
        v4u a[16];
        for (int spin = 0; spin < SPIN_CAP; ++spin) {
          FENCE();
#pragma unroll
          for (int i = 0; i < 16; ++i) a[i] = ld16(rs, e4o + i * 65536);
          unsigned acc = 0u;
#pragma unroll
          for (int i = 0; i < 16; ++i) a[i].x = strip(acc, a[i].x, t4);
          if (allok(acc)) break;
        }
        f32x4 s0 = asf(a[0]), s1 = asf(a[1]);
#pragma unroll
        for (int i = 2; i < 16; i += 2) { s0 += asf(a[i]); s1 += asf(a[i + 1]); }
        at = red4_345(s0 + s1);
      }
      STAMP(10);
      const float x2 = rb(xv + rb(bfhi(ga) * rb(at)));
      if ((lane & 8) == 0) st4(rs, e5a, tg(x2, t5));
      STAMP(11);
      for (int spin = 0; spin < SPIN_CAP; ++spin) { FENCE(); gb = ld4(rs, elo + 3072 * 4); if (allok(gb ^ tgb)) break; }
      float mo;
      {
        v4u a[7];
        for (int spin = 0; spin < SPIN_CAP; ++spin) {
          FENCE();
#pragma unroll
          for (int i = 0; i < 7; ++i) a[i] = ld16(rs, e6o + i * 65536);
          unsigned acc = 0u;
#pragma unroll
          for (int i = 0; i < 7; ++i) a[i].x = strip(acc, a[i].x, t6);
          if (allok(acc)) break;
        }
        const f32x4 s = ((asf(a[0]) + asf(a[1])) + (asf(a[2]) + asf(a[3]))) + ((asf(a[4]) + asf(a[5])) + asf(a[6]));
        mo = red4_345(s);
      }
      const float ov = x2 + rb(bfhi(gb) * rb(rb(mo) + b2v));
      if ((lane & 8) == 0) ((GAS u16*)p.out)[rb_ * 1024 + col] = (u16)pk2(ov, ov);
      if (j == 0 && lane == 0) {
        const v2u e = *(const LAS v2u*)(L + L_EP);
        const u64 e1 = (((u64)e.y << 32) | e.x) + 1;
        st8(rs, WS_EPOCH, v2u{(unsigned)e1, (unsigned)(e1 >> 32)});
      }
      STAMP(12);
    }
  } else {
    const int j = bid - 128, kh = j & 1, cg = j >> 1;
    unsigned T;
    {
      const int eb = tid >> 8, ek = (tid & 255) * 2;
      const int e1o = WS_E1 + (eb * 2048 + kh * 512 + ek) * 4;
      const int e2a = WS_E2 + ((kh * 2 + ((lane >> 4) & 1)) * 4096 + cg * 64 + (lane & 15) * 4) * 4;
      LAS char* act = L + L_ACT;
      f32x2 xn;
      VMCNT(8);
      {
        const v4u xa = *(const LAS v4u*)(L + L_XC + w * 2048 + lane * 32), xb = *(const LAS v4u*)(L + L_XC + w * 2048 + lane * 32 + 16);
        const unsigned own = *(const LAS unsigned*)(L + L_XC + w * 2048 + (kh * 512 + ek) * 2);
        float v[16];
#pragma unroll
        for (int i = 0; i < 4; ++i) { v[2 * i] = bflo(xa[i]); v[2 * i + 1] = bfhi(xa[i]); v[8 + 2 * i] = bflo(xb[i]); v[9 + 2 * i] = bfhi(xb[i]); }
        float s = 0.f;
#pragma unroll
        for (int i = 0; i < 16; ++i) s += v[i];
        s += dpp<XOR1>(s); s += dpp<XOR2>(s); s += dpp<ROR4>(s); s += dpp<ROR8>(s); s = sum32(sum16(s));
        const float mean = s * (1.f / 1024.f);
        float q = 0.f;
#pragma unroll
        for (int i = 0; i < 16; ++i) { const float d = v[i] - mean; q += d * d; }
        q += dpp<XOR1>(q); q += dpp<XOR2>(q); q += dpp<ROR4>(q); q += dpp<ROR8>(q); q = sum32(sum16(q));
        const float mb = rb(mean), rd = ln_rden(q * (1.f / 1024.f));
        xn = rb2(rb2(up2(own) - mb) * rd);
      }
      BAR();
      T = make_tag(L);
      const unsigned t1 = tagb(T, e1o) & 0xFFu, t2 = tagb(T, e2a);
      VMCNT(0);
      v4u bf[8];
#pragma unroll
      for (int ks = 0; ks < 2; ++ks)
#pragma unroll
        for (int ct = 0; ct < 4; ++ct) bf[ks * 4 + ct] = col_frag(L + w * 8192, ks, ct, lane);
#pragma unroll
      for (int i = 0; i < 8; ++i) asm volatile("" : "+v"(bf[i]));
      if (w == 0) STAMP(1);
      wait_until(t0, D_PE1);
      {
        v2u a[4], d[4];
        for (int spin = 0; spin < SPIN_CAP; ++spin) {
          FENCE();
#pragma unroll
          for (int q = 0; q < 4; ++q) { a[q] = ld8(rs, e1o + q * 16384); d[q] = ld8(rs, e1o + q * 16384 + 4096); }
          unsigned acc = 0u;
#pragma unroll
          for (int q = 0; q < 4; ++q) { a[q].x = strip(acc, a[q].x, t1); d[q].x = strip(acc, d[q].x, t1); }
          if (allok(acc)) break;
        }
        const f32x2 s = (asf2(a[0]) + asf2(a[1])) + (asf2(a[2]) + asf2(a[3]));
        const f32x2 cc = (asf2(d[0]) + asf2(d[1])) + (asf2(d[2]) + asf2(d[3]));
        const f32x2 h = rb2(xn * rb2(rb2(cc) + 1.f)) + rb2(s);
        *(LAS unsigned*)(act + (eb * 512 + ek) * 2) = pk2(h);
        if (w == 0) STAMP(2);
      }
      BAR();
      if (w == 0) STAMP(9);
      {
        const v4u a0 = act_frag(act, 512, w * 64, lane), a1 = act_frag(act, 512, w * 64 + 32, lane);
        f32x4 d[4];
#pragma unroll
        for (int ct = 0; ct < 4; ++ct) d[ct] = mfma(a1, bf[4 + ct], mfma(a0, bf[ct], f32x4{0.f, 0.f, 0.f, 0.f}));
        col_red_store(red, w, lane, d);
      }
      BAR();
      if (w == 0) {
        const f32x4 s = col_red_sum_all(red, lane);
        if (lane < 32) st16(rs, e2a, v4u{tg(s.x, t2), f2u(s.y), tg(s.z, t2 >> 16), f2u(s.w)});
        STAMP(3);
      }
    }
    if (j < 64) {
      const int eb = tid >> 8, k0 = (tid & 255) * 4;
      const v2u cv = *(const GAS v2u*)(p.c + eb * 1024 + k0);
      {
        const f32x2 c0 = up2(cv.x), c1 = up2(cv.y);
        *(LAS v2u*)(L + L_ACT + (eb * 1024 + k0) * 2) = v2u{pk2(silu_rb(c0.x), silu_rb(c0.y)), pk2(silu_rb(c1.x), silu_rb(c1.y))};
      }
      wait_until(t0, j < 16 ? D_LA : (j < 48 ? D_LB : D_LC));
      dma_col<16>(p.wmod + 2048 + j * 64, w * 128, 12288, lbase + w * 16384, lane);
      const int ela = WS_EL + ((w >> 2) * 4096 + j * 64 + (w & 3) * 16 + (lane & 7) * 2) * 4;
      const unsigned tl = tagb(T, ela);
      VMCNT(0);
      BAR();
      f32x4 d[4];
#pragma unroll
      for (int ct = 0; ct < 4; ++ct) d[ct] = f32x4{0.f, 0.f, 0.f, 0.f};
#pragma unroll
      for (int ks = 0; ks < 4; ++ks) {
        const v4u a = act_frag(L + L_ACT, 1024, w * 128 + ks * 32, lane);
#pragma unroll
        for (int ct = 0; ct < 4; ++ct) d[ct] = mfma(a, col_frag(L + w * 16384, ks, ct, lane), d[ct]);
      }
      col_red_store(red, w, lane, d);
      BAR();
      const f32x2 s = rb2(col_red_sum(red, w, lane));
      if (lane < 8) st8(rs, ela, v2u{tg(s.x, tl), tg(s.y, tl >> 8)});
      if (w == 0) STAMP(4);
    } else if (j < 120) {
      const int m = j - 64;
      const unsigned b1r = *(const GAS unsigned*)(p.b1 + m * 64 + (w & 3) * 16 + (lane & 7) * 2);
      v4u w1f[16], w2f[16];
      {
        const LAS char* img = L + w * 16384;
        const unsigned dst = lbase + w * 16384;
        const char* s2 = (const char*)p.w2 + (size_t)(m * 64 + g) * 2048 + w * 256;
        wait_until(t0, D_M);
#pragma unroll
        for (int i = 0; i < 16; ++i) dma16(s2 + (size_t)i * 4 * 2048 + row_src_off(i, lane), dst + i * 1024);
        VMCNT(8);
#pragma unroll
        for (int ct = 0; ct < 8; ++ct) w2f[ct] = row_frag(img, 0, ct, lane);
#pragma unroll
        for (int ct = 0; ct < 8; ++ct) asm volatile("" : "+v"(w2f[ct]));
        asm volatile("s_waitcnt lgkmcnt(0)" ::: "memory");
        dma_col<8>(p.w1 + m * 64, w * 128, 7168, dst, lane);
        VMCNT(8);
#pragma unroll
        for (int ct = 0; ct < 8; ++ct) w2f[8 + ct] = row_frag(img, 1, ct, lane);
#pragma unroll
        for (int ct = 0; ct < 8; ++ct) asm volatile("" : "+v"(w2f[8 + ct]));
        asm volatile("s_waitcnt lgkmcnt(0)" ::: "memory");
        dma_col<8>(p.w1 + m * 64, w * 128 + 64, 7168, dst + 8192, lane);
        VMCNT(0);
#pragma unroll
        for (int ks = 0; ks < 4; ++ks)
#pragma unroll
          for (int ct = 0; ct < 4; ++ct) w1f[ks * 4 + ct] = col_frag(img, ks, ct, lane);
#pragma unroll
        for (int i = 0; i < 16; ++i) asm volatile("" : "+v"(w1f[i]));
        if (w == 0) STAMP(12);
      }
      const int bb = (tid >> 2) & 1, k0 = (tid >> 3) * 16 + (tid & 3) * 4;
      const int elo = WS_EL + (bb * 4096 + 1024 + k0) * 4;
      const int e5o = WS_E5 + tid * 16;
      const int e6a = WS_E6 + (m * 64 + 8 * w) * 128 + (g & 1) * 64 + c * 4;
      const unsigned e6m = (c & 3) == 0 ? 0xFFFFFF00u : 0xFFFFFFFFu, e6t = (c & 3) == 0 ? (tagb(T, e6a) & 0xFFu) : 0u;
      const unsigned tl = tagb(T, elo), t5 = tagb(T, e5o);
      LAS float* lns = (LAS float*)(L + L_LN);
      LAS float* lnw = lns + (bb * 2 + (lane >> 4)) * 8 + w;
      const LAS f32x4* lnr = (const LAS f32x4*)(lns + bb * 16);
      f32x2 sb0, sb1, cb0, cb1;
      {
        v4u sbr, cbr;
        for (int spin = 0; spin < SPIN_CAP; ++spin) {
          FENCE();
          sbr = ld16(rs, elo); cbr = ld16(rs, elo + 4096);
          if (allok(chk(chk(chk(chk(0u, sbr.x, tl), cbr.x, tl), sbr.w, tl >> 24), cbr.w, tl >> 24))) break;
        }
        sb0 = f32x2{bfhi(sbr.x), bfhi(sbr.y)}; sb1 = f32x2{bfhi(sbr.z), bfhi(sbr.w)};
        cb0 = rb2(f32x2{bfhi(cbr.x), bfhi(cbr.y)} + 1.f); cb1 = rb2(f32x2{bfhi(cbr.z), bfhi(cbr.w)} + 1.f);
      }
      f32x2 v0, v1;
      {
        v4u xv;
        for (int spin = 0; spin < SPIN_CAP; ++spin) {
          FENCE();
          xv = ld16(rs, e5o);
          if (allok(chk(chk(chk(chk(0u, xv.x, t5), xv.y, t5 >> 8), xv.z, t5 >> 16), xv.w, t5 >> 24))) break;
        }
        v0 = f32x2{bfhi(xv.x), bfhi(xv.y)}; v1 = f32x2{bfhi(xv.z), bfhi(xv.w)};
      }
      if (w == 0) STAMP(5);
      float mb, rd;
      auto stats = [&](int stage) {
        const f32x2 s2 = v0 + v1, q2 = v0 * v0 + v1 * v1;
        float s = s2.x + s2.y, q = q2.x + q2.y;
        s += dpp<XOR1>(s); q += dpp<XOR1>(q);
        s += dpp<XOR2>(s); q += dpp<XOR2>(q);
        s += dpp<ROR8>(s); q += dpp<ROR8>(q);
        const float t = sum32(fold16(s, q));
        if ((lane & ~20) == 0) lnw[stage * 32] = t;
        BAR();
        const f32x4 a = lnr[stage * 8] + lnr[stage * 8 + 1], e = lnr[stage * 8 + 2] + lnr[stage * 8 + 3];
        const float mean = ((a.x + a.y) + (a.z + a.w)) * (1.f / 1024.f);
        const float var = fmaf(-mean, mean, ((e.x + e.y) + (e.z + e.w)) * (1.f / 1024.f));
        mb = rb(mean); rd = ln_rden(fmaxf(var, 0.f));
      };
      stats(0);
      if (w == 0) STAMP(13);
      v0 = rb2(rb2(rb2(rb2(v0 - mb) * rd) * cb0) + sb0);
      v1 = rb2(rb2(rb2(rb2(v1 - mb) * rd) * cb1) + sb1);
      stats(1);
      if (w == 0) STAMP(14);
      *(LAS v2u*)(L + L_ACT + (bb * 1024 + k0) * 2) = v2u{pk2(rb2(v0 - mb) * rd), pk2(rb2(v1 - mb) * rd)};
      BAR();
      if (w == 0) STAMP(6);
      {
        v4u a[4];
#pragma unroll
        for (int ks = 0; ks < 4; ++ks) a[ks] = act_frag(L + L_ACT, 1024, w * 128 + ks * 32, lane);
        f32x4 d[4];
#pragma unroll
        for (int ct = 0; ct < 4; ++ct) d[ct] = mfma(a[0], w1f[ct], f32x4{0.f, 0.f, 0.f, 0.f});
#pragma unroll
        for (int ks = 1; ks < 4; ++ks)
#pragma unroll
          for (int ct = 0; ct < 4; ++ct) d[ct] = mfma(a[ks], w1f[ks * 4 + ct], d[ct]);
        col_red_store(red, w, lane, d);
      }
      BAR();
      if (w == 0) STAMP(15);
      {
        const f32x2 z = rb2(rb2(col_red_sum(red, w, lane)) + up2(b1r));
        const f32x2 u = (z + 0.044715f * z * z * z) * (-2.f * 0.7978845608028654f * LOG2E);
        const float g0 = z.x * rcp(1.f + ex2(u.x)), g1 = z.y * rcp(1.f + ex2(u.y));
        if (lane < 8) *(LAS unsigned*)(L + L_OA + ((w >> 2) * 64 + (w & 3) * 16 + lane * 2) * 2) = pk2(g0, g1);
      }
      BAR();
      if (w == 0) STAMP(7);
      {
        const v4u a0 = act_frag(L + L_OA, 64, 0, lane), a1 = act_frag(L + L_OA, 64, 32, lane);
        f32x4 d[8];
#pragma unroll
        for (int ct = 0; ct < 8; ++ct) d[ct] = mfma(a0, w2f[ct], f32x4{0.f, 0.f, 0.f, 0.f});
#pragma unroll
        for (int ct = 0; ct < 8; ++ct) d[ct] = mfma(a1, w2f[8 + ct], d[ct]);
        if (lane < 32) {
#pragma unroll
          for (int ct = 0; ct < 8; ++ct) st4(rs, e6a + ct * 128, (f2u(d[ct].x) & e6m) | e6t);
        }
        if (w == 0) STAMP(8);
      }
    }
  }
  if (w == 0) STAMP_FLUSH();
}

}

extern "C" void kernel_launch(void* const* d_in, const int* in_sizes, int n_in, void* d_out, int out_size, void* d_ws, size_t ws_size,
                              hipStream_t stream) {
  Params p;
  p.x = (const u16*)d_in[0]; p.kc = (const u16*)d_in[1]; p.vc = (const u16*)d_in[2]; p.wq = (const u16*)d_in[3]; p.wo = (const u16*)d_in[4];
  p.w1 = (const u16*)d_in[6]; p.b1 = (const u16*)d_in[7]; p.w2 = (const u16*)d_in[8]; p.b2 = (const u16*)d_in[9];
  p.c = (const u16*)d_in[10]; p.wmod = (const u16*)d_in[11];
  p.out = (u16*)d_out; p.ws = (unsigned char*)d_ws;
  (void)hipFuncSetAttribute((const void*)fused_kernel, hipFuncAttributeMaxDynamicSharedMemorySize, LDS_BYTES);
  fused_kernel<<<256, NTHR, LDS_BYTES, stream>>>(p);
}
```

```cpp
#include <hip/hip_runtime.h>
#include <hip/hip_bf16.h>
#include <stdint.h>

namespace {

typedef unsigned short u16;
typedef unsigned long long u64;
typedef unsigned v4u __attribute__((ext_vector_type(4)));
typedef unsigned v2u __attribute__((ext_vector_type(2)));
typedef float f32x4 __attribute__((ext_vector_type(4)));
typedef float f32x2 __attribute__((ext_vector_type(2)));
typedef __bf16 bf16x8 __attribute__((ext_vector_type(8)));
typedef __bf16 bf16x2 __attribute__((ext_vector_type(2)));
typedef short s16x4 __attribute__((ext_vector_type(4)));
#define GAS __attribute__((address_space(1)))
#define LAS __attribute__((address_space(3)))

constexpr int NTHR = 512;
constexpr u64 TAGK = 0x9E3779B97F4A7C15ull, TAGC = 0x0E56CD77ull;
constexpr int SPIN_CAP = 1 << 16;

constexpr int WS_EPOCH = 0;
constexpr int WS_E1 = 8192;
constexpr int WS_E2 = WS_E1 + 65536;
constexpr int WS_E3 = WS_E2 + 131072;
constexpr int WS_E3S = WS_E3 + 524288;
constexpr int WS_E4 = WS_E3S + 16384;
constexpr int WS_E5 = WS_E4 + 524288;
constexpr int WS_EL = WS_E5 + 24576;
constexpr int WS_E6 = WS_EL + 524288;
constexpr int WS_END = WS_E6 + 458752;
constexpr int WS_SIZE = WS_END;

constexpr int L_K = 0, L_V = 65536;
constexpr int L_XC = 98304;
constexpr int L_ACT = 131072;
constexpr int L_RED = L_ACT + 4096;
constexpr int L_QS = L_RED + 4096;
constexpr int L_PA = L_QS + 2304;
constexpr int L_ML = L_PA + 8192;
constexpr int L_OA = L_ML + 512;
constexpr int L_LA = L_OA + 256;
constexpr int L_LN = L_LA + 2048;
constexpr int L_PAIR = L_LN + 256;
constexpr int L_EP = L_PAIR + 16;
constexpr int L_PF = L_EP + 1024;
constexpr int LDS_BYTES = L_PF + 1024;

#define D_Y 30
#define D_K 100
#define D_PE1 150
#define D_WO 0
#define D_M 0
#define LATE_W 0
#define LATE_A 0
#define EARLY_W 0
#define LATE_POS 1
#define PRETOUCH 0
#define XMAP 0
#define D_LATE 0
#define SP1 0
#define SP2 0
#define SP3 0
#define ST_SC 0
#define ST_GB 0
#define MWIN 16
#define PF_SLICES 11

struct Params {
  const u16 *x, *kc, *vc, *wq, *wo, *w1, *b1, *w2, *b2, *c, *wmod;
  u16* out;
  unsigned char* ws;
};

__device__ __forceinline__ u64 rt() { return __builtin_amdgcn_s_memtime(); }
template <int D> __device__ __forceinline__ void wait_until(u64 t0) { if (D > 0) { while ((long long)(rt() - t0) < (long long)D * 21) {} } }
__device__ __forceinline__ float u2f(unsigned u) { return __builtin_bit_cast(float, u); }
__device__ __forceinline__ unsigned f2u(float f) { return __builtin_bit_cast(unsigned, f); }
__device__ __forceinline__ float bflo(unsigned b) { return u2f(b << 16); }
__device__ __forceinline__ float bfhi(unsigned b) { return u2f(b & 0xFFFF0000u); }
__device__ __forceinline__ unsigned pk2(float lo, float hi) { f32x2 v = {lo, hi}; return __builtin_bit_cast(unsigned, __builtin_convertvector(v, bf16x2)); }
__device__ __forceinline__ unsigned pk2(f32x2 v) { return __builtin_bit_cast(unsigned, __builtin_convertvector(v, bf16x2)); }
__device__ __forceinline__ f32x2 up2(unsigned b) { return f32x2{bflo(b), bfhi(b)}; }
__device__ __forceinline__ f32x2 rb2(f32x2 v) { return up2(pk2(v)); }
__device__ __forceinline__ float rb(float f) { return bfhi(pk2(f, f)); }
__device__ __forceinline__ unsigned tagb(unsigned T, int A) { return __builtin_amdgcn_alignbit(T, T, 8 * (((A >> 2) + (A >> 4)) & 3)); }
__device__ __forceinline__ unsigned tg(float f, unsigned tb) { return (f2u(f) & 0xFFFFFF00u) | (tb & 0xFFu); }
__device__ __forceinline__ unsigned chk(unsigned acc, unsigned w, unsigned tb) { return acc | (w ^ tb); }
__device__ __forceinline__ unsigned tag2(unsigned T, int A) { return T >> (2 * ((A >> 2) & 15)); }
__device__ __forceinline__ unsigned tg2(float f, unsigned t) { return (f2u(f) & ~3u) | (t & 3u); }
__device__ __forceinline__ bool allok2(unsigned acc) { return __builtin_amdgcn_ballot_w64((acc & 3u) != 0u) == 0ull; }
__device__ __forceinline__ bool allok(unsigned acc) { return __builtin_amdgcn_ballot_w64((acc & 0xFFu) != 0u) == 0ull; }
__device__ __forceinline__ float vmax(float a, float b) { float r; asm("v_max_f32 %0, %1, %2" : "=v"(r) : "v"(a), "v"(b)); return r; }
__device__ __forceinline__ float vmax3(float a, float b, float c) { float r; asm("v_max3_f32 %0, %1, %2, %3" : "=v"(r) : "v"(a), "v"(b), "v"(c)); return r; }
__device__ __forceinline__ void keep(unsigned v) { asm volatile("" ::"v"(v)); }
__device__ __forceinline__ float rcp(float x) { return __builtin_amdgcn_rcpf(x); }
__device__ __forceinline__ float ex2(float x) { return __builtin_amdgcn_exp2f(x); }
constexpr float LOG2E = 1.4426950408889634f;
__device__ __forceinline__ f32x4 mfma(v4u a, v4u b, f32x4 c) {
  return __builtin_amdgcn_mfma_f32_16x16x32_bf16(__builtin_bit_cast(bf16x8, a), __builtin_bit_cast(bf16x8, b), c, 0, 0, 0);
}
#define BAR() asm volatile("s_waitcnt lgkmcnt(0)\n\ts_barrier" ::: "memory")
#define VMCNT(n) asm volatile("s_waitcnt vmcnt(" #n ")" ::: "memory")
#define SB() __builtin_amdgcn_sched_barrier(0)
#define FENCE() asm volatile("" ::: "memory")
constexpr int SC1 = 16;
typedef __amdgpu_buffer_rsrc_t rsrc_t;
__device__ __forceinline__ v4u ld16(rsrc_t rs, int off) { return __builtin_amdgcn_raw_buffer_load_b128(rs, off, 0, SC1); }
__device__ __forceinline__ v2u ld8(rsrc_t rs, int off) { return __builtin_amdgcn_raw_buffer_load_b64(rs, off, 0, SC1); }
__device__ __forceinline__ unsigned ld4(rsrc_t rs, int off) { return __builtin_amdgcn_raw_buffer_load_b32(rs, off, 0, SC1); }
__device__ __forceinline__ void st16(rsrc_t rs, int off, v4u v) { __builtin_amdgcn_raw_buffer_store_b128(v, rs, off, 0, SC1); }
__device__ __forceinline__ void st8(rsrc_t rs, int off, v2u v) { __builtin_amdgcn_raw_buffer_store_b64(v, rs, off, 0, SC1); }
__device__ __forceinline__ void st4(rsrc_t rs, int off, unsigned v) { __builtin_amdgcn_raw_buffer_store_b32(v, rs, off, 0, SC1); }
__device__ __forceinline__ v4u ldnt(const void* p) { return __builtin_nontemporal_load((const GAS v4u*)p); }
__device__ __forceinline__ f32x4 asf(v4u v) { return __builtin_bit_cast(f32x4, v); }
__device__ __forceinline__ f32x2 asf2(v2u v) { return __builtin_bit_cast(f32x2, v); }
__device__ __forceinline__ v4u tg1(f32x4 v, unsigned T, int A) { return v4u{tg(v.x, tagb(T, A)), f2u(v.y), f2u(v.z), f2u(v.w)}; }
__device__ __forceinline__ unsigned strip(unsigned& acc, unsigned w, unsigned tb8) { const unsigned x = w ^ tb8; acc |= x; return x; }
__device__ __forceinline__ v4u tg4(f32x4 v, unsigned T, int A) { const unsigned t = tagb(T, A); return v4u{tg(v.x, t), tg(v.y, t >> 8), tg(v.z, t >> 16), tg(v.w, t >> 24)}; }

template <int CTRL> __device__ __forceinline__ float dpp(float x) {
  return u2f((unsigned)__builtin_amdgcn_mov_dpp((int)f2u(x), CTRL, 0xf, 0xf, true));
}
constexpr int XOR1 = 0xB1, XOR2 = 0x4E, ROR8 = 0x128, ROR4 = 0x124;
__device__ __forceinline__ float fold16(float a, float b) { auto s = __builtin_amdgcn_permlane16_swap(f2u(a), f2u(b), false, false); const unsigned p = s[0], q = s[1]; return u2f(p) + u2f(q); }
__device__ __forceinline__ float fold32(float a, float b) { auto s = __builtin_amdgcn_permlane32_swap(f2u(a), f2u(b), false, false); const unsigned p = s[0], q = s[1]; return u2f(p) + u2f(q); }
__device__ __forceinline__ float sum16(float x) { return fold16(x, x); }
__device__ __forceinline__ float sum32(float x) { return fold32(x, x); }
__device__ __forceinline__ float max16(float x) { auto s = __builtin_amdgcn_permlane16_swap(f2u(x), f2u(x), false, false); const unsigned p = s[0], q = s[1]; return vmax(u2f(p), u2f(q)); }
__device__ __forceinline__ float max32(float x) { auto s = __builtin_amdgcn_permlane32_swap(f2u(x), f2u(x), false, false); const unsigned p = s[0], q = s[1]; return vmax(u2f(p), u2f(q)); }
__device__ __forceinline__ float red4_345(f32x4 v) {
  const float a = v.x + dpp<ROR8>(v.x), b = v.y + dpp<ROR8>(v.y), c = v.z + dpp<ROR8>(v.z), d = v.w + dpp<ROR8>(v.w);
  return fold32(fold16(a, b), fold16(c, d));
}
__device__ __forceinline__ int comp4(int lane) { return lane >> 4; }

#define STAMP(i) do {} while (0)
#define STAMP_FLUSH() do {} while (0)

__device__ __forceinline__ void dma16(const void* gsrc, unsigned ldsaddr) {
  unsigned keep;
  asm volatile("s_mov_b32 %0, m0\n\ts_mov_b32 m0, %2\n\ts_nop 0\n\tglobal_load_lds_dwordx4 %1, off nt\n\ts_mov_b32 m0, %0"
               : "=&s"(keep) : "v"(gsrc), "s"(ldsaddr) : "memory");
}
__device__ __forceinline__ void dma16_keep(const void* gsrc, unsigned ldsaddr) {
  unsigned keep;
  asm volatile("s_mov_b32 %0, m0\n\ts_mov_b32 m0, %2\n\ts_nop 0\n\tglobal_load_lds_dwordx4 %1, off\n\ts_mov_b32 m0, %0"
               : "=&s"(keep) : "v"(gsrc), "s"(ldsaddr) : "memory");
}
template <int NI>
__device__ __forceinline__ void dma_col(const u16* base, int row0, int stride_b, unsigned lds, int lane) {
  const int r = lane >> 3;
  const char* src = (const char*)base + (size_t)(row0 + r) * stride_b;
  const int c0 = ((lane & 7) ^ (((r >> 1) & 1) << 1)) * 16, c1 = c0 ^ 64;
#pragma unroll
  for (int i = 0; i < NI; ++i) dma16(src + (size_t)i * 8 * stride_b + ((i & 1) ? c1 : c0), lds + i * 1024);
}
__device__ __forceinline__ v4u tr_frag(const LAS char* p0, const LAS char* p1) {
  const v2u u0 = __builtin_bit_cast(v2u, __builtin_amdgcn_ds_read_tr16_b64_v4i16((LAS s16x4*)p0));
  const v2u u1 = __builtin_bit_cast(v2u, __builtin_amdgcn_ds_read_tr16_b64_v4i16((LAS s16x4*)p1));
  return v4u{u0.x, u0.y, u1.x, u1.y};
}
__device__ __forceinline__ v4u col_frag(const LAS char* img, int ks, int ct, int lane) {
  const int g = lane >> 4, q = (lane & 15) >> 2, pp = lane & 3;
  const int sw = ((q >> 1) & 1) | ((g & 1) << 1);
  const LAS char* p = img + (ks * 32 + 8 * g + q) * 128 + ((ct ^ sw) * 32) + pp * 8;
  return tr_frag(p, p + 512);
}
__device__ __forceinline__ int row_src_off(int i, int lane) { return ((lane & 15) ^ (2 * (lane >> 4)) ^ (((i >> 1) & 1) << 3)) * 16; }
__device__ __forceinline__ size_t next_row(int j, int i) { return j < 64 ? (size_t)((i >> 2) * 128 + (i & 3) * 4) : (size_t)(i * 4); }
__device__ __forceinline__ v4u row_frag(const LAS char* img, int ks, int ct, int lane) {
  const int g = lane >> 4, q = (lane & 15) >> 2, pp = lane & 3;
  const LAS char* p = img + (ks * 32 + 8 * g + q) * 256 + ((ct ^ (q | ((g & 1) << 2))) * 32) + pp * 8;
  return tr_frag(p, p + 1024);
}
__device__ __forceinline__ v4u act_frag(const LAS char* act, int kstr, int k0, int lane) {
  return *(const LAS v4u*)(act + (((lane >> 2) & 1) * kstr + k0 + (lane >> 4) * 8) * 2);
}
__device__ __forceinline__ void col_red_store(LAS float* red, int w, int lane, const f32x4 (&d)[4]) {
  if (lane < 32) {
    LAS float* p = red + w * 128 + (lane >> 4) * 64 + (lane & 15);
    p[0] = d[0].x; p[16] = d[1].x; p[32] = d[2].x; p[48] = d[3].x;
  }
}
__device__ __forceinline__ f32x4 col_red_sum_all(const LAS float* red, int lane) {
  const LAS f32x4* pr = (const LAS f32x4*)(red + (lane >> 5) * 4 * 128 + (lane & 31) * 4);
  f32x4 s = (pr[0] + pr[32]) + (pr[64] + pr[96]);
  const float a = fold32(s.x, s.y), b = fold32(s.z, s.w);
  auto sa = __builtin_amdgcn_permlane32_swap(f2u(a), f2u(a), false, false), sb = __builtin_amdgcn_permlane32_swap(f2u(b), f2u(b), false, false);
  const unsigned x = sa[0], y = sa[1], z = sb[0], ww = sb[1];
  return f32x4{u2f(x), u2f(y), u2f(z), u2f(ww)};
}
__device__ __forceinline__ f32x2 col_red_sum(const LAS float* red, int w, int lane) {
  f32x2 s = *(const LAS f32x2*)(red + (lane >> 3) * 128 + w * 16 + (lane & 7) * 2);
  s.x += dpp<ROR8>(s.x); s.y += dpp<ROR8>(s.y);
  s.x = sum16(s.x); s.y = sum16(s.y);
  s.x = sum32(s.x); s.y = sum32(s.y);
  return s;
}
__device__ __forceinline__ float silu_rb(float v) { return rb(v * rcp(1.f + ex2(-LOG2E * v))); }
__device__ __forceinline__ float ln_rden(float var) { return rcp(rb(__builtin_amdgcn_sqrtf(rb(rb(var) + 1e-5f)))); }
__device__ __forceinline__ unsigned make_tag(const LAS char* L) {
  const v2u e = *(const LAS v2u*)(L + L_EP);
  const u64 epoch = ((u64)__builtin_amdgcn_readfirstlane(e.y) << 32) | (unsigned)__builtin_amdgcn_readfirstlane(e.x);
  return (unsigned)(((epoch + TAGC) * TAGK) >> 32);
}

__global__ void __launch_bounds__(NTHR, 2) fused_kernel(Params p) {
  extern __shared__ __attribute__((aligned(16))) char smem[];
  LAS char* L = (LAS char*)smem;
  const u64 t0 = rt();
  const int tid = threadIdx.x, lane = tid & 63, w = __builtin_amdgcn_readfirstlane(tid >> 6), bid = blockIdx.x;
  const unsigned lbase = (unsigned)(size_t)L;
  const bool isX = bid < 128;
  const int jj = bid & 127;
  {
    const int r = isX ? lane >> 3 : lane >> 4;
    const int row0 = (jj & 3) * 256 + w * 32, stride = isX ? 12288 : 8192;
    const char* base = isX ? (const char*)(p.wmod + (jj >> 2) * 64) : (const char*)(p.wq + (jj >> 2) * 128);
    const char* src = base + (size_t)(row0 + r) * stride;
    const int c0 = isX ? ((lane & 7) ^ (((r >> 1) & 1) << 1)) * 16 : ((lane & 15) ^ (2 * r)) * 16;
    const unsigned dst = lbase + (isX ? L_V + w * 4096 : w * 8192);
    if (w == 0) dma16_keep(p.ws + WS_EPOCH + lane * 16, lbase + L_EP);
    if (PRETOUCH && w == 1) {
      const int ln = min(bid * 64 + lane, (WS_END - WS_E1) / 128 - 1);
      dma16_keep(p.ws + WS_E1 + (size_t)ln * 128, lbase + L_PF);
    }
    if (w >= 6) {
      u64 pc;
      asm volatile("s_getpc_b64 %0" : "=s"(pc));
      const int sl = (bid >> 3) & 15;
      if (sl < PF_SLICES) dma16_keep((const char*)(pc & ~255ull) + sl * 2048 + (w - 6) * 1024 + lane * 16, lbase + L_PF);
    }
    if (isX) {
      dma16(p.c + ((lane >> 2) & 1) * 1024 + row0 + (lane & 3) * 8, lbase + L_XC + w * 2048);
      dma16(p.c + ((lane >> 2) & 1) * 1024 + (jj & 15) * 64 + (w & 1) * 32 + (lane & 3) * 8, lbase + L_XC + w * 2048 + 1024);
#pragma unroll
      for (int i = 0; i < 4; ++i) dma16(src + (size_t)i * 8 * stride + (c0 ^ ((i & 1) * 64)), dst + i * 1024);
    } else {
#pragma unroll
      for (int i = 0; i < 4; ++i) dma16(p.x + i * 512 + lane * 8, lbase + L_XC + i * 1024);
      wait_until<D_Y>(t0);
#pragma unroll
      for (int i = 0; i < 8; ++i) dma16(src + (size_t)i * 4 * stride + (c0 ^ (((i >> 1) & 1) * 128)), dst + i * 1024);
    }
  }
  const rsrc_t rs = __builtin_amdgcn_make_buffer_rsrc((void*)p.ws, 0, WS_SIZE, 0x00020000);
  LAS float* red = (LAS float*)(L + L_RED);
  const int g = lane >> 4, c = lane & 15;
  if (w == 0) STAMP(0);

  if (__builtin_expect(isX, 1)) {
    const int j = jj, kq = j & 3, cg = j >> 2;
    const int b = j >> 6, hkv = (j >> 4) & 3, sp = j & 15;
    LAS char* act = L + L_ACT + w * 128;
    LAS char* lact = L + L_LA + w * 256;
    VMCNT(4);
    unsigned T = 0;
    {
      const unsigned craw = *(const LAS u16*)(L + L_XC + w * 2048 + lane * 2);
      const unsigned c2 = *(const LAS u16*)(L + L_XC + w * 2048 + 1024 + lane * 2);
      const float s = silu_rb(bflo(craw)), s2 = silu_rb(bflo(c2));
      *(LAS u16*)(act + lane * 2) = (u16)pk2(s, s);
      *(LAS u16*)(lact + lane * 2) = (u16)pk2(s2, s2);
    }
    if (tid < 4) ((volatile LAS unsigned*)(L + L_PAIR))[tid] = 0u;
    if (w == 0) T = make_tag(L);
    asm volatile("s_waitcnt lgkmcnt(0)" ::: "memory");
    const v4u a1 = act_frag(act, 32, 0, lane);
    const int e1a = WS_E1 + ((kq * 2 + ((lane >> 4) & 1)) * 2048 + cg * 64 + (lane & 15) * 4) * 4;
    const unsigned tb1 = tag2(T, e1a);
    VMCNT(0);
    {
      f32x4 d[4];
#pragma unroll
      for (int ct = 0; ct < 4; ++ct) d[ct] = mfma(a1, col_frag(L + L_V + w * 4096, 0, ct, lane), f32x4{0.f, 0.f, 0.f, 0.f});
      col_red_store(red, w, lane, d);
    }
    if (w == 0) STAMP(1);
    BAR();
    if (w == 0) {
      const f32x4 s = col_red_sum_all(red, lane);
      if (lane < 32) st16(rs, e1a, v4u{tg2(s.x, tb1), tg2(s.y, tb1 >> 2), tg2(s.z, tb1 >> 4), tg2(s.w, tb1 >> 6)});
      STAMP(2);
    } else T = make_tag(L);
    BAR();
    {
      const size_t kvoff = ((size_t)(b * 4 + hkv) * 32768 + (32768 - 4096) + sp * 256 + w * 32) * 256;
      const char* ksrc = (const char*)p.kc + kvoff + g * 256;
      const char* vsrc = (const char*)p.vc + kvoff + g * 256;
      wait_until<D_K>(t0);
#pragma unroll
      for (int i = 0; i < 8; ++i) dma16(ksrc + i * 1024 + ((c ^ ((i * 4 + g) & 15)) * 16), lbase + L_K + w * 8192 + i * 1024);
#pragma unroll
      for (int i = 0; i < 8; ++i) dma16(vsrc + i * 1024 + ((c ^ (((i * 4 + g) & 7) << 1)) * 16), lbase + L_V + w * 8192 + i * 1024);
    }
    const LAS char* qp = L + L_QS + (c & 7) * 272 + g * 16;
    LAS float* mlw = (LAS float*)(L + L_ML) + (c & 7) * 8;
    const int e3oa = WS_E3 + (((b * 4 + hkv) * 4) * 16 + sp) * 1024 + w * 128 + (g & 1) * 64 + c * 4;
    const unsigned e3m = (c & 3) == 0 ? 0xFFFFFF00u : 0xFFFFFFFFu, e3t = (c & 3) == 0 ? (tagb(T, e3oa) & 0xFFu) : 0u;
    const int e3sa = WS_E3S + ((b * 4 + hkv) * 16 + sp) * 128 + c * 8;
    const unsigned tb3s = tagb(T, e3sa);
    const int qa = WS_E2 + ((g * 2 + b) * 4096 + (hkv * 8 + w) * 128 + c * 8) * 4;
    const unsigned tq = tag2(T, qa);
    LAS char* qd = L + L_QS + w * 272 + (c * 8 + g) * 2;
    const char* lsrc = (const char*)(p.wmod + 2048 + (w >> 1) * 1024 + (j >> 4) * 128) + (size_t)((j & 15) * 64 + (w & 1) * 32 + g) * 12288;
    auto late_dma = [&](int lo, int hi) {
#pragma unroll
      for (int i = 0; i < 8; ++i) if (i >= lo && i < hi) dma16(lsrc + (size_t)i * 4 * 12288 + row_src_off(i, lane), lbase + L_K + w * 8192 + i * 1024);
    };
    VMCNT(8);
    if (w == 0) STAMP(5);
    v4u kf[8];
#pragma unroll
    for (int Tt = 0; Tt < 2; ++Tt)
#pragma unroll
      for (int s = 0; s < 4; ++s) kf[Tt * 4 + s] = *(const LAS v4u*)(L + L_K + w * 8192 + (Tt * 16 + c) * 256 + (((s * 4 + g) ^ c) * 16));
#pragma unroll
    for (int i = 0; i < 8; ++i) asm volatile("" : "+v"(kf[i]));
    if (w < LATE_W) {
      asm volatile("s_waitcnt lgkmcnt(0)" ::: "memory");
      late_dma(0, 8);
      VMCNT(8);
    } else VMCNT(0);
    if (w == 0) STAMP(6);
    BAR();
    v4u vf[8];
    {
      const int q = c >> 2, pp = c & 3, r0 = 4 * g + q;
      const LAS char* vb = L + L_V + r0 * 256 + ((w ^ (r0 & 7)) * 32) + pp * 8;
#pragma unroll
      for (int ks = 0; ks < 8; ++ks) vf[ks] = tr_frag(vb + ks * 8192, vb + ks * 8192 + 4096);
    }
#pragma unroll
    for (int i = 0; i < 8; ++i) asm volatile("" : "+v"(vf[i]));
    {
      v4u v0, v1;
      for (int spin = 0; spin < SPIN_CAP; ++spin) {
        FENCE();
        v0 = ld16(rs, qa); v1 = ld16(rs, qa + 16);
        unsigned acc = 0u;
        v0.x = strip(acc, v0.x, tq & 3u); v0.y = strip(acc, v0.y, (tq >> 2) & 3u); v0.z = strip(acc, v0.z, (tq >> 4) & 3u); v0.w = strip(acc, v0.w, (tq >> 6) & 3u);
        v1.x = strip(acc, v1.x, (tq >> 8) & 3u); v1.y = strip(acc, v1.y, (tq >> 10) & 3u); v1.z = strip(acc, v1.z, (tq >> 12) & 3u); v1.w = strip(acc, v1.w, (tq >> 14) & 3u);
        if (allok2(acc)) break;
      }
      const float q0 = fold32(fold16(u2f(v0.x), u2f(v0.y)), fold16(u2f(v0.z), u2f(v0.w)));
      const float q1 = fold32(fold16(u2f(v1.x), u2f(v1.y)), fold16(u2f(v1.z), u2f(v1.w)));
      const unsigned pq = pk2(q0, q1);
      *(LAS u16*)qd = (u16)pq; *(LAS u16*)(qd + 8) = (u16)(pq >> 16);
      if (w == 0) STAMP(3);
    }
    if (w >= LATE_W) late_dma(0, SP1);
    BAR();
    if (w == 0) STAMP(4);
    f32x4 sc[2];
    {
      v4u qf[4];
#pragma unroll
      for (int s = 0; s < 4; ++s) qf[s] = *(const LAS v4u*)(qp + s * 64);
      SB();
      f32x4 d0 = mfma(kf[0], qf[0], f32x4{0.f, 0.f, 0.f, 0.f}), d1 = mfma(kf[4], qf[0], f32x4{0.f, 0.f, 0.f, 0.f});
#pragma unroll
      for (int s = 1; s < 4; ++s) { d0 = mfma(kf[s], qf[s], d0); d1 = mfma(kf[4 + s], qf[s], d1); }
      sc[0] = d0; sc[1] = d1;
    }
    {
      const f32x2 sca = {0.08838834764831845f, 0.08838834764831845f};
#pragma unroll
      for (int Tt = 0; Tt < 2; ++Tt) {
        const f32x2 lo = rb2(rb2(f32x2{sc[Tt].x, sc[Tt].y}) * sca), hi = rb2(rb2(f32x2{sc[Tt].z, sc[Tt].w}) * sca);
        sc[Tt] = f32x4{lo.x, lo.y, hi.x, hi.y};
      }
      float mx = vmax(vmax3(vmax3(sc[0].x, sc[0].y, sc[0].z), sc[0].w, sc[1].x), vmax3(sc[1].y, sc[1].z, sc[1].w));
      mx = max32(max16(mx));
      if (lane < 8) mlw[w] = mx;
    }
    if (w >= LATE_W) late_dma(SP1, SP2);
    if (w == 0) STAMP(15);
    BAR();
    float M;
    {
      const f32x4 m0 = *(const LAS f32x4*)mlw, m1 = *(const LAS f32x4*)(mlw + 4);
      M = vmax(vmax3(vmax3(m0.x, m0.y, m0.z), m0.w, m1.x), vmax3(m1.y, m1.z, m1.w));
      const float nm = -M * LOG2E;
#pragma unroll
      for (int Tt = 0; Tt < 2; ++Tt)
#pragma unroll
        for (int r = 0; r < 4; ++r) sc[Tt][r] = ex2(fmaf(sc[Tt][r], LOG2E, nm));
      *(LAS v4u*)(L + L_PA + w * 1024 + lane * 16) = v4u{pk2(sc[0].x, sc[0].y), pk2(sc[0].z, sc[0].w), pk2(sc[1].x, sc[1].y), pk2(sc[1].z, sc[1].w)};
      const f32x4 t4 = sc[0] + sc[1];
      const float ls = sum32(sum16((t4.x + t4.y) + (t4.z + t4.w)));
      if (lane < 8) mlw[64 + w] = ls;
    }
    if (w >= LATE_W) late_dma(SP2, SP3);
    BAR();
    if (w == 0) STAMP(14);
    {
      v4u pa[8];
#pragma unroll
      for (int ks = 0; ks < 8; ++ks) pa[ks] = *(const LAS v4u*)(L + L_PA + ks * 1024 + lane * 16);
      SB();
      f32x4 o0 = mfma(pa[0], vf[0], f32x4{0.f, 0.f, 0.f, 0.f}), o1 = mfma(pa[1], vf[1], f32x4{0.f, 0.f, 0.f, 0.f});
#pragma unroll
      for (int ks = 2; ks < 8; ks += 2) { o0 = mfma(pa[ks], vf[ks], o0); o1 = mfma(pa[ks + 1], vf[ks + 1], o1); }
      const f32x4 o = o0 + o1;
      if (lane < 32) {
#pragma unroll
        for (int r = 0; r < 4; ++r) st4(rs, e3oa + r * 16384, (f2u(o[r]) & e3m) | e3t);
      }
      if (w == 7) {
        const f32x4 l4 = *(const LAS f32x4*)(mlw + 64) + *(const LAS f32x4*)(mlw + 68);
        if (lane < 16) st8(rs, e3sa, v2u{tg(M, tb3s), tg((l4.x + l4.y) + (l4.z + l4.w), tb3s >> 8)});
      }
      if (w == 0) STAMP(7);
    }
    {
      const int ela = WS_EL + (((w >> 1) * 16 + (j & 15)) * 2 + ((lane >> 4) & 1)) * 4096 + ((j >> 4) * 128 + (lane >> 5) * 64 + (lane & 15) * 4) * 4;
      const unsigned tl = tagb(T, ela);
      const v4u a0 = act_frag(lact, 32, 0, lane);
      BAR();
      if (w >= LATE_W) {
        if (ST_GB > 0 && w >= 2) { const u64 tb = rt(); const long long dl = (w >= 6 ? ST_GB : ST_SC) * 21; while ((long long)(rt() - tb) < dl) {} }
        late_dma(SP3, 8);
        VMCNT(0);
      }
      f32x4 d[8];
#pragma unroll
      for (int ct = 0; ct < 8; ++ct) d[ct] = mfma(a0, row_frag(L + L_K + w * 8192, 0, ct, lane), f32x4{0.f, 0.f, 0.f, 0.f});
      LAS float* rp = (LAS float*)(L + L_V) + w * 256;
      if (lane < 32) {
#pragma unroll
        for (int ct = 0; ct < 8; ++ct) rp[g * 128 + ct * 16 + c] = d[ct].x;
      }
      volatile LAS unsigned* pf = (volatile LAS unsigned*)(L + L_PAIR) + (w >> 1);
      asm volatile("s_waitcnt lgkmcnt(0)" ::: "memory");
      if (w & 1) {
        if (lane == 0) *pf = 1u;
      } else {
        for (int spin = 0; spin < (1 << 22) && *pf == 0u; ++spin) {}
        asm volatile("" ::: "memory");
        const LAS f32x4* pr = (const LAS f32x4*)(rp + ((lane >> 4) & 1) * 128 + (lane >> 5) * 64 + (lane & 15) * 4);
        const f32x4 sm = pr[0] + pr[64];
        st16(rs, ela, v4u{tg(sm.x, tl), f2u(sm.y), f2u(sm.z), f2u(sm.w)});
        if (w == 0) STAMP(8);
      }
    }
  } else {
    const int j = jj, kq4 = j & 3, hd = j >> 2;
    unsigned T;
    const char* nsrc = (j < 64 ? (const char*)p.wo + (size_t)((j >> 3) * 512 + (j & 7) * 16 + g) * 2048 : (const char*)p.w2 + (size_t)((j - 64) * 64 + g) * 2048) + w * 256;
    {
      const int eb = lane >> 5, ek = kq4 * 256 + w * 32 + (lane & 31);
      const int e1o = WS_E1 + (eb * 2048 + ek) * 4;
      const int e2a = WS_E2 + ((kq4 * 2 + (w >> 2)) * 4096 + hd * 128 + (w & 3) * 32 + (lane & 7) * 4 + comp4(lane)) * 4;
      LAS char* act = L + L_ACT + w * 128;
      LAS float* red2 = (LAS float*)(L + L_PA);
      float xn;
      VMCNT(8);
      {
        const LAS v4u* xr = (const LAS v4u*)(L + L_XC + eb * 2048 + (lane & 31) * 64);
        const unsigned own = *(const LAS u16*)(L + L_XC + (eb * 1024 + ek) * 2);
        float v[32];
#pragma unroll
        for (int q4 = 0; q4 < 4; ++q4) {
          const v4u xa = xr[q4];
#pragma unroll
          for (int i = 0; i < 4; ++i) { v[q4 * 8 + 2 * i] = bflo(xa[i]); v[q4 * 8 + 2 * i + 1] = bfhi(xa[i]); }
        }
        float s = 0.f;
#pragma unroll
        for (int i = 0; i < 32; ++i) s += v[i];
        s += dpp<XOR1>(s); s += dpp<XOR2>(s); s += dpp<ROR4>(s); s += dpp<ROR8>(s); s = sum16(s);
        const float mean = s * (1.f / 1024.f);
        float q = 0.f;
#pragma unroll
        for (int i = 0; i < 32; ++i) { const float d = v[i] - mean; q += d * d; }
        q += dpp<XOR1>(q); q += dpp<XOR2>(q); q += dpp<ROR4>(q); q += dpp<ROR8>(q); q = sum16(q);
        const float mb = rb(mean), rd = ln_rden(q * (1.f / 1024.f));
        xn = rb(rb(bflo(own) - mb) * rd);
      }
      VMCNT(0);
      v4u bf[8];
#pragma unroll
      for (int ct = 0; ct < 8; ++ct) bf[ct] = row_frag(L + w * 8192, 0, ct, lane);
#pragma unroll
      for (int i = 0; i < 8; ++i) asm volatile("" : "+v"(bf[i]));
      BAR();
      T = make_tag(L);
      const unsigned t1 = tag2(T, e1o) & 3u, t2 = tag2(T, e2a);
      if (j < 120) {
#pragma unroll
        for (int i = 0; i < EARLY_W; ++i) dma16(nsrc + next_row(j, i) * 2048 + row_src_off(i, lane), lbase + w * 16384 + i * 1024);
      }
      if (w == 0) STAMP(1);
      wait_until<D_PE1>(t0);
      {
        unsigned a[4], d[4];
        for (int spin = 0; spin < SPIN_CAP; ++spin) {
          FENCE();
#pragma unroll
          for (int q = 0; q < 4; ++q) { a[q] = ld4(rs, e1o + q * 16384); d[q] = ld4(rs, e1o + q * 16384 + 4096); }
          unsigned acc = 0u;
#pragma unroll
          for (int q = 0; q < 4; ++q) { a[q] = strip(acc, a[q], t1); d[q] = strip(acc, d[q], t1); }
          if (allok2(acc)) break;
        }
        const float s = (u2f(a[0]) + u2f(a[1])) + (u2f(a[2]) + u2f(a[3]));
        const float cc = (u2f(d[0]) + u2f(d[1])) + (u2f(d[2]) + u2f(d[3]));
        const float h = rb(xn * rb(rb(cc) + 1.f)) + rb(s);
        *(LAS u16*)(act + lane * 2) = (u16)pk2(h, h);
        if (w == 0) STAMP(2);
      }
      asm volatile("s_waitcnt lgkmcnt(0)" ::: "memory");
      {
        const v4u a0 = act_frag(act, 32, 0, lane);
        f32x4 d[8];
#pragma unroll
        for (int ct = 0; ct < 8; ++ct) d[ct] = mfma(a0, bf[ct], f32x4{0.f, 0.f, 0.f, 0.f});
        if (lane < 32) {
          LAS float* pr = red2 + w * 256 + g * 128 + c;
#pragma unroll
          for (int ct = 0; ct < 8; ++ct) pr[ct * 16] = d[ct].x;
        }
      }
      BAR();
      if (w == 0) STAMP(9);
      {
        const f32x4 pv = *(const LAS f32x4*)(red2 + (lane >> 3) * 256 + (w >> 2) * 128 + (w & 3) * 32 + (lane & 7) * 4);
        const float s = red4_345(pv);
        if ((lane & 8) == 0) st4(rs, e2a, tg2(s, t2));
        if (w == 0) STAMP(3);
      }
      BAR();
    }
    if (j < 64) {
      const int hq = j >> 3, d16 = j & 7;
      v4u wf[16];
      wait_until<D_WO>(t0);
#pragma unroll
      for (int i = EARLY_W; i < 16; ++i) dma16(nsrc + (size_t)((i >> 2) * 128 + (i & 3) * 4) * 2048 + row_src_off(i, lane), lbase + w * 16384 + i * 1024);
      const int bb = w >> 2, hh = w & 3;
      const int oo = WS_E3 + (((bb * 4 + (hq >> 1)) * 4 + hh) * 16 + (lane >> 2)) * 1024 + d16 * 128 + (hq & 1) * 64 + (lane & 3) * 16;
      const int so = WS_E3S + ((bb * 4 + (hq >> 1)) * 16 + (lane >> 2)) * 128 + ((hq & 1) * 4 + hh) * 8;
      const unsigned to = tagb(T, oo) & 0xFFu, ts0 = tagb(T, so) & 0xFFu, ts1 = (tagb(T, so) >> 8) & 0xFFu;
      const int e4a = WS_E4 + (j * 64 + 8 * w) * 128 + (g & 1) * 64 + c * 4;
      const unsigned e4m = (c & 3) == 0 ? 0xFFFFFF00u : 0xFFFFFFFFu, e4t = (c & 3) == 0 ? (tagb(T, e4a) & 0xFFu) : 0u;
      const int p8 = lane >> 3, ch = lane & 7, rbb = ch >> 2, col = j * 16 + (ch & 3) * 4 + comp4(lane);
      const int elo = WS_EL + (p8 * 2 + rbb) * 4096 + (j * 16 + (ch & 3) * 4) * 4;
      const int e4o = WS_E4 + (p8 * 64 + j) * 128 + ch * 16, e6o = WS_E6 + (p8 * 64 + j) * 128 + ch * 16;
      const int e5a = WS_E5 + j * 128 + ch * 16 + comp4(lane) * 4;
      const unsigned tel = tagb(T, elo) & 0xFFu, t4 = tagb(T, e4o) & 0xFFu, t6 = tagb(T, e6o) & 0xFFu, t5 = tagb(T, e5a);
      float xv = 0.f, b2v = 0.f;
      if (w == 0) { xv = bflo(p.x[rbb * 1024 + col]); b2v = bflo(p.b2[col]); }
      VMCNT(0);
#pragma unroll
      for (int ks = 0; ks < 2; ++ks)
#pragma unroll
        for (int ct = 0; ct < 8; ++ct) wf[ks * 8 + ct] = row_frag(L + w * 16384, ks, ct, lane);
#pragma unroll
      for (int i = 0; i < 16; ++i) asm volatile("" : "+v"(wf[i]));
      if (w == 0) STAMP(4);
      {
        v4u o4; v2u s2;
        for (int spin = 0; spin < SPIN_CAP; ++spin) {
          FENCE();
          o4 = ld16(rs, oo); s2 = ld8(rs, so);
          unsigned acc = 0u;
          o4.x = strip(acc, o4.x, to); s2.x = strip(acc, s2.x, ts0); s2.y = strip(acc, s2.y, ts1);
          if (allok(acc)) break;
        }
        if (w == 0) STAMP(5);
        const float m = u2f(s2.x);
        float Mx = vmax(m, dpp<ROR4>(m));
        Mx = vmax(Mx, dpp<ROR8>(Mx)); Mx = max32(max16(Mx));
        const float f = ex2((m - Mx) * LOG2E);
        f32x4 o = f * asf(o4);
        float l = f * u2f(s2.y);
        o.x += dpp<ROR4>(o.x); o.y += dpp<ROR4>(o.y); o.z += dpp<ROR4>(o.z); o.w += dpp<ROR4>(o.w); l += dpp<ROR4>(l);
        l += dpp<ROR8>(l); l = sum32(sum16(l));
        const float ov = red4_345(o) * rcp(l);
        if ((lane & 12) == 0) *(LAS u16*)(L + L_OA + (bb * 64 + hh * 16 + (lane & 3) * 4 + comp4(lane)) * 2) = (u16)pk2(ov, ov);
      }
      BAR();
      if (w == 0) STAMP(6);
      {
        const v4u a0 = act_frag(L + L_OA, 64, 0, lane), a1 = act_frag(L + L_OA, 64, 32, lane);
        f32x4 d[8];
#pragma unroll
        for (int ct = 0; ct < 8; ++ct) d[ct] = mfma(a0, wf[ct], f32x4{0.f, 0.f, 0.f, 0.f});
#pragma unroll
        for (int ct = 0; ct < 8; ++ct) d[ct] = mfma(a1, wf[8 + ct], d[ct]);
        if (lane < 32) {
#pragma unroll
          for (int ct = 0; ct < 8; ++ct) st4(rs, e4a + ct * 128, (f2u(d[ct].x) & e4m) | e4t);
        }
        if (w == 0) STAMP(7);
      }
      if (w == 1) {
        {
          v4u a[4];
          for (int spin = 0; spin < SPIN_CAP; ++spin) {
            FENCE();
#pragma unroll
            for (int i = 0; i < 4; ++i) a[i] = ld16(rs, elo + (2 + i) * 65536);
            unsigned acc = 0u;
#pragma unroll
            for (int i = 0; i < 4; ++i) a[i].x = strip(acc, a[i].x, tel);
            if (allok(acc)) break;
          }
          const float sbv = rb(red4_345(asf(a[0]) + asf(a[1]))), cbv = rb(1.f + rb(red4_345(asf(a[2]) + asf(a[3]))));
          if ((lane & 8) == 0) { st4(rs, e5a + 8192, tg(sbv, t5)); st4(rs, e5a + 16384, tg(cbv, t5)); }
        }
      }
      if (w == 0) {
        float at, gav;
        {
          v4u a[8], e, e2;
          for (int spin = 0; spin < SPIN_CAP; ++spin) {
            FENCE();
#pragma unroll
            for (int i = 0; i < 8; ++i) a[i] = ld16(rs, e4o + i * 65536);
            e = ld16(rs, elo); e2 = ld16(rs, elo + 65536);
            unsigned acc = 0u;
#pragma unroll
            for (int i = 0; i < 8; ++i) a[i].x = strip(acc, a[i].x, t4);
            e.x = strip(acc, e.x, tel); e2.x = strip(acc, e2.x, tel);
            if (allok(acc)) break;
          }
          at = red4_345(((asf(a[0]) + asf(a[1])) + (asf(a[2]) + asf(a[3]))) + ((asf(a[4]) + asf(a[5])) + (asf(a[6]) + asf(a[7]))));
          gav = rb(red4_345(asf(e) + asf(e2)));
        }
        STAMP(10);
        const float x2 = rb(xv + rb(gav * rb(at)));
        if ((lane & 8) == 0) st4(rs, e5a, tg(x2, t5));
        STAMP(11);
        float gbv;
        {
          v4u e, e2;
          for (int spin = 0; spin < SPIN_CAP; ++spin) {
            FENCE();
            e = ld16(rs, elo + 6 * 65536); e2 = ld16(rs, elo + 7 * 65536);
            unsigned acc = 0u;
            e.x = strip(acc, e.x, tel); e2.x = strip(acc, e2.x, tel);
            if (allok(acc)) break;
          }
          gbv = rb(red4_345(asf(e) + asf(e2)));
        }
        float mo;
        {
          v4u a[7];
          for (int spin = 0; spin < SPIN_CAP; ++spin) {
            FENCE();
#pragma unroll
            for (int i = 0; i < 7; ++i) a[i] = ld16(rs, e6o + i * 65536);
            unsigned acc = 0u;
#pragma unroll
            for (int i = 0; i < 7; ++i) a[i].x = strip(acc, a[i].x, t6);
            if (allok(acc)) break;
          }
          mo = red4_345(((asf(a[0]) + asf(a[1])) + (asf(a[2]) + asf(a[3]))) + ((asf(a[4]) + asf(a[5])) + asf(a[6])));
        }
        const float ov = x2 + rb(gbv * rb(rb(mo) + b2v));
        if ((lane & 8) == 0) ((GAS u16*)p.out)[rbb * 1024 + col] = (u16)pk2(ov, ov);
        if (j == 0 && lane == 0) {
          const v2u e = *(const LAS v2u*)(L + L_EP);
          const u64 e1 = (((u64)e.y << 32) | e.x) + 1;
          st8(rs, WS_EPOCH, v2u{(unsigned)e1, (unsigned)(e1 >> 32)});
        }
        STAMP(12);
      }
    } else if (j < 120) {
      const int m = j - 64;
      const unsigned b1r = *(const GAS unsigned*)(p.b1 + m * 64 + (w & 3) * 16 + (lane & 7) * 2);
      const int bb = (tid >> 2) & 1, k0 = (tid >> 3) * 16 + (tid & 3) * 4;
      const int e5o = WS_E5 + tid * 16;
      v4u w1f[16], w2f[16];
      {
        const LAS char* img = L + w * 16384;
        const unsigned dst = lbase + w * 16384;
#define PACE() asm volatile("s_waitcnt vmcnt(%0)" ::"i"(MWIN - 1) : "memory")
        const int r8 = lane >> 3;
        const char* s1 = (const char*)(p.w1 + m * 64) + (size_t)(w * 128 + r8) * 7168;
        const int c0 = ((lane & 7) ^ (((r8 >> 1) & 1) << 1)) * 16;
        wait_until<D_M>(t0);
#pragma unroll
        for (int i = EARLY_W; i < 16; ++i) { if (i >= MWIN) PACE(); dma16(nsrc + (size_t)i * 4 * 2048 + row_src_off(i, lane), dst + i * 1024); }
        asm volatile("s_waitcnt vmcnt(%0)" ::"i"(MWIN < 8 ? MWIN - 1 : 8) : "memory");
#pragma unroll
        for (int ct = 0; ct < 8; ++ct) w2f[ct] = row_frag(img, 0, ct, lane);
#pragma unroll
        for (int ct = 0; ct < 8; ++ct) asm volatile("" : "+v"(w2f[ct]));
        asm volatile("s_waitcnt lgkmcnt(0)" ::: "memory");
#pragma unroll
        for (int i = 0; i < 8; ++i) { PACE(); dma16(s1 + (size_t)i * 8 * 7168 + (c0 ^ ((i & 1) * 64)), dst + i * 1024); }
        asm volatile("s_waitcnt vmcnt(%0)" ::"i"(MWIN < 8 ? MWIN - 1 : 8) : "memory");
#pragma unroll
        for (int ct = 0; ct < 8; ++ct) w2f[8 + ct] = row_frag(img, 1, ct, lane);
#pragma unroll
        for (int ct = 0; ct < 8; ++ct) asm volatile("" : "+v"(w2f[8 + ct]));
        asm volatile("s_waitcnt lgkmcnt(0)" ::: "memory");
#pragma unroll
        for (int i = 8; i < 16; ++i) { PACE(); dma16(s1 + (size_t)i * 8 * 7168 + (c0 ^ ((i & 1) * 64)), dst + i * 1024); }
        VMCNT(0);
#pragma unroll
        for (int ks = 0; ks < 4; ++ks)
#pragma unroll
          for (int ct = 0; ct < 4; ++ct) w1f[ks * 4 + ct] = col_frag(img, ks, ct, lane);
#pragma unroll
        for (int i = 0; i < 16; ++i) asm volatile("" : "+v"(w1f[i]));
        if (w == 0) STAMP(12);
      }
      const int e6a = WS_E6 + (m * 64 + 8 * w) * 128 + (g & 1) * 64 + c * 4;
      const unsigned e6m = (c & 3) == 0 ? 0xFFFFFF00u : 0xFFFFFFFFu, e6t = (c & 3) == 0 ? (tagb(T, e6a) & 0xFFu) : 0u;
      const unsigned t5 = tagb(T, e5o);
      LAS float* lns = (LAS float*)(L + L_LN);
      LAS float* lnw = lns + (bb * 2 + (lane >> 4)) * 8 + w;
      const LAS f32x4* lnr = (const LAS f32x4*)(lns + bb * 16);
      f32x2 v0, v1;
      {
        v4u xv;
        for (int spin = 0; spin < SPIN_CAP; ++spin) {
          FENCE();
          xv = ld16(rs, e5o);
          if (allok(chk(chk(chk(chk(0u, xv.x, t5), xv.y, t5 >> 8), xv.z, t5 >> 16), xv.w, t5 >> 24))) break;
        }
        v0 = f32x2{bfhi(xv.x), bfhi(xv.y)}; v1 = f32x2{bfhi(xv.z), bfhi(xv.w)};
      }
      v4u sbr = ld16(rs, e5o + 8192), cbr = ld16(rs, e5o + 16384);
      if (w == 0) STAMP(5);
      float mb, rd;
      auto stats = [&](int stage) {
        const f32x2 s2 = v0 + v1, q2 = v0 * v0 + v1 * v1;
        float s = s2.x + s2.y, q = q2.x + q2.y;
        s += dpp<XOR1>(s); q += dpp<XOR1>(q);
        s += dpp<XOR2>(s); q += dpp<XOR2>(q);
        s += dpp<ROR8>(s); q += dpp<ROR8>(q);
        const float t = sum32(fold16(s, q));
        if ((lane & ~20) == 0) lnw[stage * 32] = t;
        BAR();
        const f32x4 a = lnr[stage * 8] + lnr[stage * 8 + 1], e = lnr[stage * 8 + 2] + lnr[stage * 8 + 3];
        const float mean = ((a.x + a.y) + (a.z + a.w)) * (1.f / 1024.f);
        const float var = fmaf(-mean, mean, ((e.x + e.y) + (e.z + e.w)) * (1.f / 1024.f));
        mb = rb(mean); rd = ln_rden(fmaxf(var, 0.f));
      };
      stats(0);
      if (w == 0) STAMP(13);
      f32x2 sb0, sb1, cb0, cb1;
      {
        for (int spin = 0; spin < SPIN_CAP; ++spin) {
          if (allok(chk(chk(chk(chk(chk(chk(chk(chk(0u, sbr.x, t5), sbr.y, t5 >> 8), sbr.z, t5 >> 16), sbr.w, t5 >> 24), cbr.x, t5), cbr.y, t5 >> 8), cbr.z, t5 >> 16), cbr.w, t5 >> 24))) break;
          FENCE();
          sbr = ld16(rs, e5o + 8192); cbr = ld16(rs, e5o + 16384);
        }
        sb0 = f32x2{bfhi(sbr.x), bfhi(sbr.y)}; sb1 = f32x2{bfhi(sbr.z), bfhi(sbr.w)};
        cb0 = f32x2{bfhi(cbr.x), bfhi(cbr.y)}; cb1 = f32x2{bfhi(cbr.z), bfhi(cbr.w)};
      }
      v0 = rb2(rb2(rb2(rb2(v0 - mb) * rd) * cb0) + sb0);
      v1 = rb2(rb2(rb2(rb2(v1 - mb) * rd) * cb1) + sb1);
      stats(1);
      if (w == 0) STAMP(14);
      *(LAS v2u*)(L + L_ACT + (bb * 1024 + k0) * 2) = v2u{pk2(rb2(v0 - mb) * rd), pk2(rb2(v1 - mb) * rd)};
      asm volatile("s_waitcnt lgkmcnt(0)" ::: "memory");
      if (w == 0) STAMP(6);
      {
        v4u a[4];
#pragma unroll
        for (int ks = 0; ks < 4; ++ks) a[ks] = act_frag(L + L_ACT, 1024, w * 128 + ks * 32, lane);
        f32x4 d[4];
#pragma unroll
        for (int ct = 0; ct < 4; ++ct) d[ct] = mfma(a[0], w1f[ct], f32x4{0.f, 0.f, 0.f, 0.f});
#pragma unroll
        for (int ks = 1; ks < 4; ++ks)
#pragma unroll
          for (int ct = 0; ct < 4; ++ct) d[ct] = mfma(a[ks], w1f[ks * 4 + ct], d[ct]);
        col_red_store(red, w, lane, d);
      }
      BAR();
      if (w == 0) STAMP(15);
      {
        const f32x2 z = rb2(rb2(col_red_sum(red, w, lane)) + up2(b1r));
        const f32x2 u = (z + 0.044715f * z * z * z) * (-2.f * 0.7978845608028654f * LOG2E);
        const float g0 = z.x * rcp(1.f + ex2(u.x)), g1 = z.y * rcp(1.f + ex2(u.y));
        if (lane < 8) *(LAS unsigned*)(L + L_OA + ((w >> 2) * 64 + (w & 3) * 16 + lane * 2) * 2) = pk2(g0, g1);
      }
      BAR();
      if (w == 0) STAMP(7);
      {
        const v4u a0 = act_frag(L + L_OA, 64, 0, lane), a1 = act_frag(L + L_OA, 64, 32, lane);
        f32x4 d[8];
#pragma unroll
        for (int ct = 0; ct < 8; ++ct) d[ct] = mfma(a0, w2f[ct], f32x4{0.f, 0.f, 0.f, 0.f});
#pragma unroll
        for (int ct = 0; ct < 8; ++ct) d[ct] = mfma(a1, w2f[8 + ct], d[ct]);
        if (lane < 32) {
#pragma unroll
          for (int ct = 0; ct < 8; ++ct) st4(rs, e6a + ct * 128, (f2u(d[ct].x) & e6m) | e6t);
        }
        if (w == 0) STAMP(8);
      }
    }
  }
  if (w == 0) STAMP_FLUSH();
}

}

extern "C" void kernel_launch(void* const* d_in, const int* in_sizes, int n_in, void* d_out, int out_size, void* d_ws, size_t ws_size,
                              hipStream_t stream) {
  Params p;
  p.x = (const u16*)d_in[0]; p.kc = (const u16*)d_in[1]; p.vc = (const u16*)d_in[2]; p.wq = (const u16*)d_in[3]; p.wo = (const u16*)d_in[4];
  p.w1 = (const u16*)d_in[6]; p.b1 = (const u16*)d_in[7]; p.w2 = (const u16*)d_in[8]; p.b2 = (const u16*)d_in[9];
  p.c = (const u16*)d_in[10]; p.wmod = (const u16*)d_in[11];
  p.out = (u16*)d_out; p.ws = (unsigned char*)d_ws;
  (void)hipFuncSetAttribute((const void*)fused_kernel, hipFuncAttributeMaxDynamicSharedMemorySize, LDS_BYTES);
  fused_kernel<<<256, NTHR, LDS_BYTES, stream>>>(p);
}
```

```cpp
#include <hip/hip_runtime.h>
#include <hip/hip_bf16.h>
#include <stdint.h>

namespace {

typedef unsigned short u16;
typedef unsigned long long u64;
typedef unsigned v4u __attribute__((ext_vector_type(4)));
typedef unsigned v2u __attribute__((ext_vector_type(2)));
typedef float f32x4 __attribute__((ext_vector_type(4)));
typedef float f32x2 __attribute__((ext_vector_type(2)));
typedef __bf16 bf16x8 __attribute__((ext_vector_type(8)));
typedef __bf16 bf16x2 __attribute__((ext_vector_type(2)));
typedef short s16x4 __attribute__((ext_vector_type(4)));
#define GAS __attribute__((address_space(1)))
#define LAS __attribute__((address_space(3)))

constexpr int NTHR = 512;
constexpr u64 TAGK = 0x9E3779B97F4A7C15ull, TAGC = 0x0E56CD77ull;
constexpr int SPIN_CAP = 1 << 16;

constexpr int WS_EPOCH = 0;
constexpr int WS_E1 = 8192;
constexpr int WS_E2 = WS_E1 + 65536;
constexpr int WS_E3 = WS_E2 + 131072;
constexpr int WS_E3S = WS_E3 + 524288;
constexpr int WS_E4 = WS_E3S + 16384;
constexpr int WS_E5 = WS_E4 + 524288;
constexpr int WS_EL = WS_E5 + 24576;
constexpr int WS_E6 = WS_EL + 524288;
constexpr int WS_END = WS_E6 + 458752;
constexpr int WS_SIZE = WS_END;

constexpr int L_K = 0, L_V = 65536;
constexpr int L_XC = 98304;
constexpr int L_ACT = 131072;
constexpr int L_RED = L_ACT + 4096;
constexpr int L_QS = L_RED + 4096;
constexpr int L_PA = L_QS + 2304;
constexpr int L_ML = L_PA + 8192;
constexpr int L_OA = L_ML + 512;
constexpr int L_LA = L_OA + 256;
constexpr int L_LN = L_LA + 2048;
constexpr int L_PAIR = L_LN + 256;
constexpr int L_EP = L_PAIR + 16;
constexpr int L_PF = L_EP + 1024;
constexpr int LDS_BYTES = L_PF + 1024;

#define D_Y 30
#define D_K 100
#define D_PE1 150
#define D_WO 0
#define D_M 0
#define LATE_W 0
#define LATE_A 0
#define EARLY_W 0
#define LATE_POS 1
#define PRETOUCH 0
#define XMAP 0
#define D_LATE 0
#define SP1 0
#define SP2 0
#define SP3 0
#define ST_SC 0
#define ST_GB 0
#define MWIN 16
#define TLB_PF 1
#define PF_SLICES 12

struct Params {
  const u16 *x, *kc, *vc, *wq, *wo, *w1, *b1, *w2, *b2, *c, *wmod;
  u16* out;
  unsigned char* ws;
};

__device__ __forceinline__ u64 rt() { return __builtin_amdgcn_s_memtime(); }
template <int D> __device__ __forceinline__ void wait_until(u64 t0) { if (D > 0) { while ((long long)(rt() - t0) < (long long)D * 21) {} } }
__device__ __forceinline__ float u2f(unsigned u) { return __builtin_bit_cast(float, u); }
__device__ __forceinline__ unsigned f2u(float f) { return __builtin_bit_cast(unsigned, f); }
__device__ __forceinline__ float bflo(unsigned b) { return u2f(b << 16); }
__device__ __forceinline__ float bfhi(unsigned b) { return u2f(b & 0xFFFF0000u); }
__device__ __forceinline__ unsigned pk2(float lo, float hi) { f32x2 v = {lo, hi}; return __builtin_bit_cast(unsigned, __builtin_convertvector(v, bf16x2)); }
__device__ __forceinline__ unsigned pk2(f32x2 v) { return __builtin_bit_cast(unsigned, __builtin_convertvector(v, bf16x2)); }
__device__ __forceinline__ f32x2 up2(unsigned b) { return f32x2{bflo(b), bfhi(b)}; }
__device__ __forceinline__ f32x2 rb2(f32x2 v) { return up2(pk2(v)); }
__device__ __forceinline__ float rb(float f) { return bfhi(pk2(f, f)); }
__device__ __forceinline__ unsigned tagb(unsigned T, int A) { return __builtin_amdgcn_alignbit(T, T, 8 * (((A >> 2) + (A >> 4)) & 3)); }
__device__ __forceinline__ unsigned tg(float f, unsigned tb) { return (f2u(f) & 0xFFFFFF00u) | (tb & 0xFFu); }
__device__ __forceinline__ unsigned chk(unsigned acc, unsigned w, unsigned tb) { return acc | (w ^ tb); }
__device__ __forceinline__ unsigned tag2(unsigned T, int A) { return T >> (2 * ((A >> 2) & 15)); }
__device__ __forceinline__ unsigned tg2(float f, unsigned t) { return (f2u(f) & ~3u) | (t & 3u); }
__device__ __forceinline__ bool allok2(unsigned acc) { return __builtin_amdgcn_ballot_w64((acc & 3u) != 0u) == 0ull; }
__device__ __forceinline__ bool allok(unsigned acc) { return __builtin_amdgcn_ballot_w64((acc & 0xFFu) != 0u) == 0ull; }
__device__ __forceinline__ float vmax(float a, float b) { float r; asm("v_max_f32 %0, %1, %2" : "=v"(r) : "v"(a), "v"(b)); return r; }
__device__ __forceinline__ float vmax3(float a, float b, float c) { float r; asm("v_max3_f32 %0, %1, %2, %3" : "=v"(r) : "v"(a), "v"(b), "v"(c)); return r; }
__device__ __forceinline__ void keep(unsigned v) { asm volatile("" ::"v"(v)); }
__device__ __forceinline__ float rcp(float x) { return __builtin_amdgcn_rcpf(x); }
__device__ __forceinline__ float ex2(float x) { return __builtin_amdgcn_exp2f(x); }
constexpr float LOG2E = 1.4426950408889634f;
__device__ __forceinline__ f32x4 mfma(v4u a, v4u b, f32x4 c) {
  return __builtin_amdgcn_mfma_f32_16x16x32_bf16(__builtin_bit_cast(bf16x8, a), __builtin_bit_cast(bf16x8, b), c, 0, 0, 0);
}
#define BAR() asm volatile("s_waitcnt lgkmcnt(0)\n\ts_barrier" ::: "memory")
#define VMCNT(n) asm volatile("s_waitcnt vmcnt(" #n ")" ::: "memory")
#define SB() __builtin_amdgcn_sched_barrier(0)
#define FENCE() asm volatile("" ::: "memory")
constexpr int SC1 = 16;
typedef __amdgpu_buffer_rsrc_t rsrc_t;
__device__ __forceinline__ v4u ld16(rsrc_t rs, int off) { return __builtin_amdgcn_raw_buffer_load_b128(rs, off, 0, SC1); }
__device__ __forceinline__ v2u ld8(rsrc_t rs, int off) { return __builtin_amdgcn_raw_buffer_load_b64(rs, off, 0, SC1); }
__device__ __forceinline__ unsigned ld4(rsrc_t rs, int off) { return __builtin_amdgcn_raw_buffer_load_b32(rs, off, 0, SC1); }
__device__ __forceinline__ void st16(rsrc_t rs, int off, v4u v) { __builtin_amdgcn_raw_buffer_store_b128(v, rs, off, 0, SC1); }
__device__ __forceinline__ void st8(rsrc_t rs, int off, v2u v) { __builtin_amdgcn_raw_buffer_store_b64(v, rs, off, 0, SC1); }
__device__ __forceinline__ void st4(rsrc_t rs, int off, unsigned v) { __builtin_amdgcn_raw_buffer_store_b32(v, rs, off, 0, SC1); }
__device__ __forceinline__ v4u ldnt(const void* p) { return __builtin_nontemporal_load((const GAS v4u*)p); }
__device__ __forceinline__ f32x4 asf(v4u v) { return __builtin_bit_cast(f32x4, v); }
__device__ __forceinline__ f32x2 asf2(v2u v) { return __builtin_bit_cast(f32x2, v); }
__device__ __forceinline__ v4u tg1(f32x4 v, unsigned T, int A) { return v4u{tg(v.x, tagb(T, A)), f2u(v.y), f2u(v.z), f2u(v.w)}; }
__device__ __forceinline__ unsigned strip(unsigned& acc, unsigned w, unsigned tb8) { const unsigned x = w ^ tb8; acc |= x; return x; }
__device__ __forceinline__ v4u tg4(f32x4 v, unsigned T, int A) { const unsigned t = tagb(T, A); return v4u{tg(v.x, t), tg(v.y, t >> 8), tg(v.z, t >> 16), tg(v.w, t >> 24)}; }

template <int CTRL> __device__ __forceinline__ float dpp(float x) {
  return u2f((unsigned)__builtin_amdgcn_mov_dpp((int)f2u(x), CTRL, 0xf, 0xf, true));
}
constexpr int XOR1 = 0xB1, XOR2 = 0x4E, ROR8 = 0x128, ROR4 = 0x124;
__device__ __forceinline__ float fold16(float a, float b) { auto s = __builtin_amdgcn_permlane16_swap(f2u(a), f2u(b), false, false); const unsigned p = s[0], q = s[1]; return u2f(p) + u2f(q); }
__device__ __forceinline__ float fold32(float a, float b) { auto s = __builtin_amdgcn_permlane32_swap(f2u(a), f2u(b), false, false); const unsigned p = s[0], q = s[1]; return u2f(p) + u2f(q); }
__device__ __forceinline__ float sum16(float x) { return fold16(x, x); }
__device__ __forceinline__ float sum32(float x) { return fold32(x, x); }
__device__ __forceinline__ float max16(float x) { auto s = __builtin_amdgcn_permlane16_swap(f2u(x), f2u(x), false, false); const unsigned p = s[0], q = s[1]; return vmax(u2f(p), u2f(q)); }
__device__ __forceinline__ float max32(float x) { auto s = __builtin_amdgcn_permlane32_swap(f2u(x), f2u(x), false, false); const unsigned p = s[0], q = s[1]; return vmax(u2f(p), u2f(q)); }
__device__ __forceinline__ float red4_345(f32x4 v) {
  const float a = v.x + dpp<ROR8>(v.x), b = v.y + dpp<ROR8>(v.y), c = v.z + dpp<ROR8>(v.z), d = v.w + dpp<ROR8>(v.w);
  return fold32(fold16(a, b), fold16(c, d));
}
__device__ __forceinline__ int comp4(int lane) { return lane >> 4; }

#define STAMP(i) do {} while (0)
#define STAMP_FLUSH() do {} while (0)

__device__ __forceinline__ void dma16(const void* gsrc, unsigned ldsaddr) {
  unsigned keep;
  asm volatile("s_mov_b32 %0, m0\n\ts_mov_b32 m0, %2\n\ts_nop 0\n\tglobal_load_lds_dwordx4 %1, off nt\n\ts_mov_b32 m0, %0"
               : "=&s"(keep) : "v"(gsrc), "s"(ldsaddr) : "memory");
}
__device__ __forceinline__ void dma16_keep(const void* gsrc, unsigned ldsaddr) {
  unsigned keep;
  asm volatile("s_mov_b32 %0, m0\n\ts_mov_b32 m0, %2\n\ts_nop 0\n\tglobal_load_lds_dwordx4 %1, off\n\ts_mov_b32 m0, %0"
               : "=&s"(keep) : "v"(gsrc), "s"(ldsaddr) : "memory");
}
template <int NI>
__device__ __forceinline__ void dma_col(const u16* base, int row0, int stride_b, unsigned lds, int lane) {
  const int r = lane >> 3;
  const char* src = (const char*)base + (size_t)(row0 + r) * stride_b;
  const int c0 = ((lane & 7) ^ (((r >> 1) & 1) << 1)) * 16, c1 = c0 ^ 64;
#pragma unroll
  for (int i = 0; i < NI; ++i) dma16(src + (size_t)i * 8 * stride_b + ((i & 1) ? c1 : c0), lds + i * 1024);
}
__device__ __forceinline__ v4u tr_frag(const LAS char* p0, const LAS char* p1) {
  const v2u u0 = __builtin_bit_cast(v2u, __builtin_amdgcn_ds_read_tr16_b64_v4i16((LAS s16x4*)p0));
  const v2u u1 = __builtin_bit_cast(v2u, __builtin_amdgcn_ds_read_tr16_b64_v4i16((LAS s16x4*)p1));
  return v4u{u0.x, u0.y, u1.x, u1.y};
}
__device__ __forceinline__ v4u col_frag(const LAS char* img, int ks, int ct, int lane) {
  const int g = lane >> 4, q = (lane & 15) >> 2, pp = lane & 3;
  const int sw = ((q >> 1) & 1) | ((g & 1) << 1);
  const LAS char* p = img + (ks * 32 + 8 * g + q) * 128 + ((ct ^ sw) * 32) + pp * 8;
  return tr_frag(p, p + 512);
}
__device__ __forceinline__ int row_src_off(int i, int lane) { return ((lane & 15) ^ (2 * (lane >> 4)) ^ (((i >> 1) & 1) << 3)) * 16; }
__device__ __forceinline__ size_t next_row(int j, int i) { return j < 64 ? (size_t)((i >> 2) * 128 + (i & 3) * 4) : (size_t)(i * 4); }
__device__ __forceinline__ v4u row_frag(const LAS char* img, int ks, int ct, int lane) {
  const int g = lane >> 4, q = (lane & 15) >> 2, pp = lane & 3;
  const LAS char* p = img + (ks * 32 + 8 * g + q) * 256 + ((ct ^ (q | ((g & 1) << 2))) * 32) + pp * 8;
  return tr_frag(p, p + 1024);
}
__device__ __forceinline__ v4u act_frag(const LAS char* act, int kstr, int k0, int lane) {
  return *(const LAS v4u*)(act + (((lane >> 2) & 1) * kstr + k0 + (lane >> 4) * 8) * 2);
}
__device__ __forceinline__ void col_red_store(LAS float* red, int w, int lane, const f32x4 (&d)[4]) {
  if (lane < 32) {
    LAS float* p = red + w * 128 + (lane >> 4) * 64 + (lane & 15);
    p[0] = d[0].x; p[16] = d[1].x; p[32] = d[2].x; p[48] = d[3].x;
  }
}
__device__ __forceinline__ f32x4 col_red_sum_all(const LAS float* red, int lane) {
  const LAS f32x4* pr = (const LAS f32x4*)(red + (lane >> 5) * 4 * 128 + (lane & 31) * 4);
  f32x4 s = (pr[0] + pr[32]) + (pr[64] + pr[96]);
  const float a = fold32(s.x, s.y), b = fold32(s.z, s.w);
  auto sa = __builtin_amdgcn_permlane32_swap(f2u(a), f2u(a), false, false), sb = __builtin_amdgcn_permlane32_swap(f2u(b), f2u(b), false, false);
  const unsigned x = sa[0], y = sa[1], z = sb[0], ww = sb[1];
  return f32x4{u2f(x), u2f(y), u2f(z), u2f(ww)};
}
__device__ __forceinline__ f32x2 col_red_sum(const LAS float* red, int w, int lane) {
  f32x2 s = *(const LAS f32x2*)(red + (lane >> 3) * 128 + w * 16 + (lane & 7) * 2);
  s.x += dpp<ROR8>(s.x); s.y += dpp<ROR8>(s.y);
  s.x = sum16(s.x); s.y = sum16(s.y);
  s.x = sum32(s.x); s.y = sum32(s.y);
  return s;
}
__device__ __forceinline__ float silu_rb(float v) { return rb(v * rcp(1.f + ex2(-LOG2E * v))); }
__device__ __forceinline__ float ln_rden(float var) { return rcp(rb(__builtin_amdgcn_sqrtf(rb(rb(var) + 1e-5f)))); }
__device__ __forceinline__ unsigned make_tag(const LAS char* L) {
  const v2u e = *(const LAS v2u*)(L + L_EP);
  const u64 epoch = ((u64)__builtin_amdgcn_readfirstlane(e.y) << 32) | (unsigned)__builtin_amdgcn_readfirstlane(e.x);
  return (unsigned)(((epoch + TAGC) * TAGK) >> 32);
}

__global__ void __launch_bounds__(NTHR, 2) fused_kernel(Params p) {
  extern __shared__ __attribute__((aligned(16))) char smem[];
  LAS char* L = (LAS char*)smem;
  const u64 t0 = rt();
  const int tid = threadIdx.x, lane = tid & 63, w = __builtin_amdgcn_readfirstlane(tid >> 6), bid = blockIdx.x;
  const unsigned lbase = (unsigned)(size_t)L;
  const bool isX = bid < 128;
  const int jj = bid & 127;
  {
    const int r = isX ? lane >> 3 : lane >> 4;
    const int row0 = (jj & 3) * 256 + w * 32, stride = isX ? 12288 : 8192;
    const char* base = isX ? (const char*)(p.wmod + (jj >> 2) * 64) : (const char*)(p.wq + (jj >> 2) * 128);
    const char* src = base + (size_t)(row0 + r) * stride;
    const int c0 = isX ? ((lane & 7) ^ (((r >> 1) & 1) << 1)) * 16 : ((lane & 15) ^ (2 * r)) * 16;
    const unsigned dst = lbase + (isX ? L_V + w * 4096 : w * 8192);
    if (w == 0) dma16_keep(p.ws + WS_EPOCH + lane * 16, lbase + L_EP);
    if (PRETOUCH && w == 1) {
      const int ln = min(bid * 64 + lane, (WS_END - WS_E1) / 128 - 1);
      dma16_keep(p.ws + WS_E1 + (size_t)ln * 128, lbase + L_PF);
    }
    if (w >= 6) {
      u64 pc;
      asm volatile("s_getpc_b64 %0" : "=s"(pc));
      const int sl = (bid >> 3) & 15;
      if (sl < PF_SLICES) dma16_keep((const char*)(pc & ~255ull) + sl * 2048 + (w - 6) * 1024 + lane * 16, lbase + L_PF);
    }
    if (isX) {
      dma16(p.c + ((lane >> 2) & 1) * 1024 + row0 + (lane & 3) * 8, lbase + L_XC + w * 2048);
      dma16(p.c + ((lane >> 2) & 1) * 1024 + (jj & 15) * 64 + (w & 1) * 32 + (lane & 3) * 8, lbase + L_XC + w * 2048 + 1024);
#pragma unroll
      for (int i = 0; i < 4; ++i) dma16(src + (size_t)i * 8 * stride + (c0 ^ ((i & 1) * 64)), dst + i * 1024);
    } else {
#pragma unroll
      for (int i = 0; i < 4; ++i) dma16(p.x + i * 512 + lane * 8, lbase + L_XC + i * 1024);
      wait_until<D_Y>(t0);
#pragma unroll
      for (int i = 0; i < 8; ++i) dma16(src + (size_t)i * 4 * stride + (c0 ^ (((i >> 1) & 1) * 128)), dst + i * 1024);
    }
    if (TLB_PF && w == 5) {
      const int r = (bid >> 3) * 32 + (lane & 31);
      const char* a;
      if (r < 128) a = (const char*)p.wo + (size_t)r * 65536;
      else if (r < 240) a = (const char*)p.w1 + (size_t)(r - 128) * 65536;
      else if (r < 352) a = (const char*)p.w2 + (size_t)(r - 240) * 65536;
      else if (r < 544) a = (const char*)p.wmod + (size_t)(r - 352) * 65536;
      else if (r < 672) a = (const char*)p.kc + ((size_t)((r - 544) >> 4) * 32768 + 28672) * 256 + (size_t)((r - 544) & 15) * 65536;
      else if (r < 800) a = (const char*)p.vc + ((size_t)((r - 672) >> 4) * 32768 + 28672) * 256 + (size_t)((r - 672) & 15) * 65536;
      else if (r < 928) a = (const char*)p.wq + (size_t)(r - 800) * 65536;
      else a = (const char*)p.ws + (size_t)min(r - 928, WS_END / 65536) * 65536;
      dma16_keep(a + (lane >> 5) * 16, lbase + L_PF);
    }
  }
  const rsrc_t rs = __builtin_amdgcn_make_buffer_rsrc((void*)p.ws, 0, WS_SIZE, 0x00020000);
  LAS float* red = (LAS float*)(L + L_RED);
  const int g = lane >> 4, c = lane & 15;
  if (w == 0) STAMP(0);

  if (__builtin_expect(isX, 1)) {
    const int j = jj, kq = j & 3, cg = j >> 2;
    const int b = j >> 6, hkv = (j >> 4) & 3, sp = j & 15;
    LAS char* act = L + L_ACT + w * 128;
    LAS char* lact = L + L_LA + w * 256;
    if (TLB_PF && w == 5) VMCNT(5); else VMCNT(4);
    unsigned T = 0;
    {
      const unsigned craw = *(const LAS u16*)(L + L_XC + w * 2048 + lane * 2);
      const unsigned c2 = *(const LAS u16*)(L + L_XC + w * 2048 + 1024 + lane * 2);
      const float s = silu_rb(bflo(craw)), s2 = silu_rb(bflo(c2));
      *(LAS u16*)(act + lane * 2) = (u16)pk2(s, s);
      *(LAS u16*)(lact + lane * 2) = (u16)pk2(s2, s2);
    }
    if (tid < 4) ((volatile LAS unsigned*)(L + L_PAIR))[tid] = 0u;
    if (w == 0) T = make_tag(L);
    asm volatile("s_waitcnt lgkmcnt(0)" ::: "memory");
    const v4u a1 = act_frag(act, 32, 0, lane);
    const int e1a = WS_E1 + ((kq * 2 + ((lane >> 4) & 1)) * 2048 + cg * 64 + (lane & 15) * 4) * 4;
    const unsigned tb1 = tag2(T, e1a);
    if (TLB_PF && w == 5) VMCNT(1); else VMCNT(0);
    {
      f32x4 d[4];
#pragma unroll
      for (int ct = 0; ct < 4; ++ct) d[ct] = mfma(a1, col_frag(L + L_V + w * 4096, 0, ct, lane), f32x4{0.f, 0.f, 0.f, 0.f});
      col_red_store(red, w, lane, d);
    }
    if (w == 0) STAMP(1);
    BAR();
    if (w == 0) {
      const f32x4 s = col_red_sum_all(red, lane);
      if (lane < 32) st16(rs, e1a, v4u{tg2(s.x, tb1), tg2(s.y, tb1 >> 2), tg2(s.z, tb1 >> 4), tg2(s.w, tb1 >> 6)});
      STAMP(2);
    } else T = make_tag(L);
    BAR();
    {
      const size_t kvoff = ((size_t)(b * 4 + hkv) * 32768 + (32768 - 4096) + sp * 256 + w * 32) * 256;
      const char* ksrc = (const char*)p.kc + kvoff + g * 256;
      const char* vsrc = (const char*)p.vc + kvoff + g * 256;
      wait_until<D_K>(t0);
#pragma unroll
      for (int i = 0; i < 8; ++i) dma16(ksrc + i * 1024 + ((c ^ ((i * 4 + g) & 15)) * 16), lbase + L_K + w * 8192 + i * 1024);
#pragma unroll
      for (int i = 0; i < 8; ++i) dma16(vsrc + i * 1024 + ((c ^ (((i * 4 + g) & 7) << 1)) * 16), lbase + L_V + w * 8192 + i * 1024);
    }
    const LAS char* qp = L + L_QS + (c & 7) * 272 + g * 16;
    LAS float* mlw = (LAS float*)(L + L_ML) + (c & 7) * 8;
    const int e3oa = WS_E3 + (((b * 4 + hkv) * 4) * 16 + sp) * 1024 + w * 128 + (g & 1) * 64 + c * 4;
    const unsigned e3m = (c & 3) == 0 ? 0xFFFFFF00u : 0xFFFFFFFFu, e3t = (c & 3) == 0 ? (tagb(T, e3oa) & 0xFFu) : 0u;
    const int e3sa = WS_E3S + ((b * 4 + hkv) * 16 + sp) * 128 + c * 8;
    const unsigned tb3s = tagb(T, e3sa);
    const int qa = WS_E2 + ((g * 2 + b) * 4096 + (hkv * 8 + w) * 128 + c * 8) * 4;
    const unsigned tq = tag2(T, qa);
    LAS char* qd = L + L_QS + w * 272 + (c * 8 + g) * 2;
    const char* lsrc = (const char*)(p.wmod + 2048 + (w >> 1) * 1024 + (j >> 4) * 128) + (size_t)((j & 15) * 64 + (w & 1) * 32 + g) * 12288;
    auto late_dma = [&](int lo, int hi) {
#pragma unroll
      for (int i = 0; i < 8; ++i) if (i >= lo && i < hi) dma16(lsrc + (size_t)i * 4 * 12288 + row_src_off(i, lane), lbase + L_K + w * 8192 + i * 1024);
    };
    VMCNT(8);
    if (w == 0) STAMP(5);
    v4u kf[8];
#pragma unroll
    for (int Tt = 0; Tt < 2; ++Tt)
#pragma unroll
      for (int s = 0; s < 4; ++s) kf[Tt * 4 + s] = *(const LAS v4u*)(L + L_K + w * 8192 + (Tt * 16 + c) * 256 + (((s * 4 + g) ^ c) * 16));
#pragma unroll
    for (int i = 0; i < 8; ++i) asm volatile("" : "+v"(kf[i]));
    if (w < LATE_W) {
      asm volatile("s_waitcnt lgkmcnt(0)" ::: "memory");
      late_dma(0, 8);
      VMCNT(8);
    } else VMCNT(0);
    if (w == 0) STAMP(6);
    BAR();
    v4u vf[8];
    {
      const int q = c >> 2, pp = c & 3, r0 = 4 * g + q;
      const LAS char* vb = L + L_V + r0 * 256 + ((w ^ (r0 & 7)) * 32) + pp * 8;
#pragma unroll
      for (int ks = 0; ks < 8; ++ks) vf[ks] = tr_frag(vb + ks * 8192, vb + ks * 8192 + 4096);
    }
#pragma unroll
    for (int i = 0; i < 8; ++i) asm volatile("" : "+v"(vf[i]));
    {
      v4u v0, v1;
      for (int spin = 0; spin < SPIN_CAP; ++spin) {
        FENCE();
        v0 = ld16(rs, qa); v1 = ld16(rs, qa + 16);
        unsigned acc = 0u;
        v0.x = strip(acc, v0.x, tq & 3u); v0.y = strip(acc, v0.y, (tq >> 2) & 3u); v0.z = strip(acc, v0.z, (tq >> 4) & 3u); v0.w = strip(acc, v0.w, (tq >> 6) & 3u);
        v1.x = strip(acc, v1.x, (tq >> 8) & 3u); v1.y = strip(acc, v1.y, (tq >> 10) & 3u); v1.z = strip(acc, v1.z, (tq >> 12) & 3u); v1.w = strip(acc, v1.w, (tq >> 14) & 3u);
        if (allok2(acc)) break;
      }
      const float q0 = fold32(fold16(u2f(v0.x), u2f(v0.y)), fold16(u2f(v0.z), u2f(v0.w)));
      const float q1 = fold32(fold16(u2f(v1.x), u2f(v1.y)), fold16(u2f(v1.z), u2f(v1.w)));
      const unsigned pq = pk2(q0, q1);
      *(LAS u16*)qd = (u16)pq; *(LAS u16*)(qd + 8) = (u16)(pq >> 16);
      if (w == 0) STAMP(3);
    }
    if (w >= LATE_W) late_dma(0, SP1);
    BAR();
    if (w == 0) STAMP(4);
    f32x4 sc[2];
    {
      v4u qf[4];
#pragma unroll
      for (int s = 0; s < 4; ++s) qf[s] = *(const LAS v4u*)(qp + s * 64);
      SB();
      f32x4 d0 = mfma(kf[0], qf[0], f32x4{0.f, 0.f, 0.f, 0.f}), d1 = mfma(kf[4], qf[0], f32x4{0.f, 0.f, 0.f, 0.f});
#pragma unroll
      for (int s = 1; s < 4; ++s) { d0 = mfma(kf[s], qf[s], d0); d1 = mfma(kf[4 + s], qf[s], d1); }
      sc[0] = d0; sc[1] = d1;
    }
    {
      const f32x2 sca = {0.08838834764831845f, 0.08838834764831845f};
#pragma unroll
      for (int Tt = 0; Tt < 2; ++Tt) {
        const f32x2 lo = rb2(rb2(f32x2{sc[Tt].x, sc[Tt].y}) * sca), hi = rb2(rb2(f32x2{sc[Tt].z, sc[Tt].w}) * sca);
        sc[Tt] = f32x4{lo.x, lo.y, hi.x, hi.y};
      }
      float mx = vmax(vmax3(vmax3(sc[0].x, sc[0].y, sc[0].z), sc[0].w, sc[1].x), vmax3(sc[1].y, sc[1].z, sc[1].w));
      mx = max32(max16(mx));
      if (lane < 8) mlw[w] = mx;
    }
    if (w >= LATE_W) late_dma(SP1, SP2);
    if (w == 0) STAMP(15);
    BAR();
    float M;
    {
      const f32x4 m0 = *(const LAS f32x4*)mlw, m1 = *(const LAS f32x4*)(mlw + 4);
      M = vmax(vmax3(vmax3(m0.x, m0.y, m0.z), m0.w, m1.x), vmax3(m1.y, m1.z, m1.w));
      const float nm = -M * LOG2E;
#pragma unroll
      for (int Tt = 0; Tt < 2; ++Tt)
#pragma unroll
        for (int r = 0; r < 4; ++r) sc[Tt][r] = ex2(fmaf(sc[Tt][r], LOG2E, nm));
      *(LAS v4u*)(L + L_PA + w * 1024 + lane * 16) = v4u{pk2(sc[0].x, sc[0].y), pk2(sc[0].z, sc[0].w), pk2(sc[1].x, sc[1].y), pk2(sc[1].z, sc[1].w)};
      const f32x4 t4 = sc[0] + sc[1];
      const float ls = sum32(sum16((t4.x + t4.y) + (t4.z + t4.w)));
      if (lane < 8) mlw[64 + w] = ls;
    }
    if (w >= LATE_W) late_dma(SP2, SP3);
    BAR();
    if (w == 0) STAMP(14);
    {
      v4u pa[8];
#pragma unroll
      for (int ks = 0; ks < 8; ++ks) pa[ks] = *(const LAS v4u*)(L + L_PA + ks * 1024 + lane * 16);
      SB();
      f32x4 o0 = mfma(pa[0], vf[0], f32x4{0.f, 0.f, 0.f, 0.f}), o1 = mfma(pa[1], vf[1], f32x4{0.f, 0.f, 0.f, 0.f});
#pragma unroll
      for (int ks = 2; ks < 8; ks += 2) { o0 = mfma(pa[ks], vf[ks], o0); o1 = mfma(pa[ks + 1], vf[ks + 1], o1); }
      const f32x4 o = o0 + o1;
      if (lane < 32) {
#pragma unroll
        for (int r = 0; r < 4; ++r) st4(rs, e3oa + r * 16384, (f2u(o[r]) & e3m) | e3t);
      }
      if (w == 7) {
        const f32x4 l4 = *(const LAS f32x4*)(mlw + 64) + *(const LAS f32x4*)(mlw + 68);
        if (lane < 16) st8(rs, e3sa, v2u{tg(M, tb3s), tg((l4.x + l4.y) + (l4.z + l4.w), tb3s >> 8)});
      }
      if (w == 0) STAMP(7);
    }
    {
      const int ela = WS_EL + (((w >> 1) * 16 + (j & 15)) * 2 + ((lane >> 4) & 1)) * 4096 + ((j >> 4) * 128 + (lane >> 5) * 64 + (lane & 15) * 4) * 4;
      const unsigned tl = tagb(T, ela);
      const v4u a0 = act_frag(lact, 32, 0, lane);
      BAR();
      if (w >= LATE_W) {
        if (ST_GB > 0 && w >= 2) { const u64 tb = rt(); const long long dl = (w >= 6 ? ST_GB : ST_SC) * 21; while ((long long)(rt() - tb) < dl) {} }
        late_dma(SP3, 8);
        VMCNT(0);
      }
      f32x4 d[8];
#pragma unroll
      for (int ct = 0; ct < 8; ++ct) d[ct] = mfma(a0, row_frag(L + L_K + w * 8192, 0, ct, lane), f32x4{0.f, 0.f, 0.f, 0.f});
      LAS float* rp = (LAS float*)(L + L_V) + w * 256;
      if (lane < 32) {
#pragma unroll
        for (int ct = 0; ct < 8; ++ct) rp[g * 128 + ct * 16 + c] = d[ct].x;
      }
      volatile LAS unsigned* pf = (volatile LAS unsigned*)(L + L_PAIR) + (w >> 1);
      asm volatile("s_waitcnt lgkmcnt(0)" ::: "memory");
      if (w & 1) {
        if (lane == 0) *pf = 1u;
      } else {
        for (int spin = 0; spin < (1 << 22) && *pf == 0u; ++spin) {}
        asm volatile("" ::: "memory");
        const LAS f32x4* pr = (const LAS f32x4*)(rp + ((lane >> 4) & 1) * 128 + (lane >> 5) * 64 + (lane & 15) * 4);
        const f32x4 sm = pr[0] + pr[64];
        st16(rs, ela, v4u{tg(sm.x, tl), f2u(sm.y), f2u(sm.z), f2u(sm.w)});
        if (w == 0) STAMP(8);
      }
    }
  } else {
    const int j = jj, kq4 = j & 3, hd = j >> 2;
    unsigned T;
    const char* nsrc = (j < 64 ? (const char*)p.wo + (size_t)((j >> 3) * 512 + (j & 7) * 16 + g) * 2048 : (const char*)p.w2 + (size_t)((j - 64) * 64 + g) * 2048) + w * 256;
    {
      const int eb = lane >> 5, ek = kq4 * 256 + w * 32 + (lane & 31);
      const int e1o = WS_E1 + (eb * 2048 + ek) * 4;
      const int e2a = WS_E2 + ((kq4 * 2 + (w >> 2)) * 4096 + hd * 128 + (w & 3) * 32 + (lane & 7) * 4 + comp4(lane)) * 4;
      LAS char* act = L + L_ACT + w * 128;
      LAS float* red2 = (LAS float*)(L + L_PA);
      float xn;
      if (TLB_PF && w == 5) VMCNT(9); else VMCNT(8);
      {
        const LAS v4u* xr = (const LAS v4u*)(L + L_XC + eb * 2048 + (lane & 31) * 64);
        const unsigned own = *(const LAS u16*)(L + L_XC + (eb * 1024 + ek) * 2);
        float v[32];
#pragma unroll
        for (int q4 = 0; q4 < 4; ++q4) {
          const v4u xa = xr[q4];
#pragma unroll
          for (int i = 0; i < 4; ++i) { v[q4 * 8 + 2 * i] = bflo(xa[i]); v[q4 * 8 + 2 * i + 1] = bfhi(xa[i]); }
        }
        float s = 0.f;
#pragma unroll
        for (int i = 0; i < 32; ++i) s += v[i];
        s += dpp<XOR1>(s); s += dpp<XOR2>(s); s += dpp<ROR4>(s); s += dpp<ROR8>(s); s = sum16(s);
        const float mean = s * (1.f / 1024.f);
        float q = 0.f;
#pragma unroll
        for (int i = 0; i < 32; ++i) { const float d = v[i] - mean; q += d * d; }
        q += dpp<XOR1>(q); q += dpp<XOR2>(q); q += dpp<ROR4>(q); q += dpp<ROR8>(q); q = sum16(q);
        const float mb = rb(mean), rd = ln_rden(q * (1.f / 1024.f));
        xn = rb(rb(bflo(own) - mb) * rd);
      }
      if (TLB_PF && w == 5) VMCNT(1); else VMCNT(0);
      v4u bf[8];
#pragma unroll
      for (int ct = 0; ct < 8; ++ct) bf[ct] = row_frag(L + w * 8192, 0, ct, lane);
#pragma unroll
      for (int i = 0; i < 8; ++i) asm volatile("" : "+v"(bf[i]));
      BAR();
      T = make_tag(L);
      const unsigned t1 = tag2(T, e1o) & 3u, t2 = tag2(T, e2a);
      if (j < 120) {
#pragma unroll
        for (int i = 0; i < EARLY_W; ++i) dma16(nsrc + next_row(j, i) * 2048 + row_src_off(i, lane), lbase + w * 16384 + i * 1024);
      }
      if (w == 0) STAMP(1);
      wait_until<D_PE1>(t0);
      {
        unsigned a[4], d[4];
        for (int spin = 0; spin < SPIN_CAP; ++spin) {
          FENCE();
#pragma unroll
          for (int q = 0; q < 4; ++q) { a[q] = ld4(rs, e1o + q * 16384); d[q] = ld4(rs, e1o + q * 16384 + 4096); }
          unsigned acc = 0u;
#pragma unroll
          for (int q = 0; q < 4; ++q) { a[q] = strip(acc, a[q], t1); d[q] = strip(acc, d[q], t1); }
          if (allok2(acc)) break;
        }
        const float s = (u2f(a[0]) + u2f(a[1])) + (u2f(a[2]) + u2f(a[3]));
        const float cc = (u2f(d[0]) + u2f(d[1])) + (u2f(d[2]) + u2f(d[3]));
        const float h = rb(xn * rb(rb(cc) + 1.f)) + rb(s);
        *(LAS u16*)(act + lane * 2) = (u16)pk2(h, h);
        if (w == 0) STAMP(2);
      }
      asm volatile("s_waitcnt lgkmcnt(0)" ::: "memory");
      {
        const v4u a0 = act_frag(act, 32, 0, lane);
        f32x4 d[8];
#pragma unroll
        for (int ct = 0; ct < 8; ++ct) d[ct] = mfma(a0, bf[ct], f32x4{0.f, 0.f, 0.f, 0.f});
        if (lane < 32) {
          LAS float* pr = red2 + w * 256 + g * 128 + c;
#pragma unroll
          for (int ct = 0; ct < 8; ++ct) pr[ct * 16] = d[ct].x;
        }
      }
      BAR();
      if (w == 0) STAMP(9);
      {
        const f32x4 pv = *(const LAS f32x4*)(red2 + (lane >> 3) * 256 + (w >> 2) * 128 + (w & 3) * 32 + (lane & 7) * 4);
        const float s = red4_345(pv);
        if ((lane & 8) == 0) st4(rs, e2a, tg2(s, t2));
        if (w == 0) STAMP(3);
      }
      BAR();
    }
    if (j < 64) {
      const int hq = j >> 3, d16 = j & 7;
      v4u wf[16];
      wait_until<D_WO>(t0);
#pragma unroll
      for (int i = EARLY_W; i < 16; ++i) dma16(nsrc + (size_t)((i >> 2) * 128 + (i & 3) * 4) * 2048 + row_src_off(i, lane), lbase + w * 16384 + i * 1024);
      const int bb = w >> 2, hh = w & 3;
      const int oo = WS_E3 + (((bb * 4 + (hq >> 1)) * 4 + hh) * 16 + (lane >> 2)) * 1024 + d16 * 128 + (hq & 1) * 64 + (lane & 3) * 16;
      const int so = WS_E3S + ((bb * 4 + (hq >> 1)) * 16 + (lane >> 2)) * 128 + ((hq & 1) * 4 + hh) * 8;
      const unsigned to = tagb(T, oo) & 0xFFu, ts0 = tagb(T, so) & 0xFFu, ts1 = (tagb(T, so) >> 8) & 0xFFu;
      const int e4a = WS_E4 + (j * 64 + 8 * w) * 128 + (g & 1) * 64 + c * 4;
      const unsigned e4m = (c & 3) == 0 ? 0xFFFFFF00u : 0xFFFFFFFFu, e4t = (c & 3) == 0 ? (tagb(T, e4a) & 0xFFu) : 0u;
      const int p8 = lane >> 3, ch = lane & 7, rbb = ch >> 2, col = j * 16 + (ch & 3) * 4 + comp4(lane);
      const int elo = WS_EL + (p8 * 2 + rbb) * 4096 + (j * 16 + (ch & 3) * 4) * 4;
      const int e4o = WS_E4 + (p8 * 64 + j) * 128 + ch * 16, e6o = WS_E6 + (p8 * 64 + j) * 128 + ch * 16;
      const int e5a = WS_E5 + j * 128 + ch * 16 + comp4(lane) * 4;
      const unsigned tel = tagb(T, elo) & 0xFFu, t4 = tagb(T, e4o) & 0xFFu, t6 = tagb(T, e6o) & 0xFFu, t5 = tagb(T, e5a);
      float xv = 0.f, b2v = 0.f;
      if (w == 0) { xv = bflo(p.x[rbb * 1024 + col]); b2v = bflo(p.b2[col]); }
      VMCNT(0);
#pragma unroll
      for (int ks = 0; ks < 2; ++ks)
#pragma unroll
        for (int ct = 0; ct < 8; ++ct) wf[ks * 8 + ct] = row_frag(L + w * 16384, ks, ct, lane);
#pragma unroll
      for (int i = 0; i < 16; ++i) asm volatile("" : "+v"(wf[i]));
      if (w == 0) STAMP(4);
      {
        v4u o4; v2u s2;
        for (int spin = 0; spin < SPIN_CAP; ++spin) {
          FENCE();
          o4 = ld16(rs, oo); s2 = ld8(rs, so);
          unsigned acc = 0u;
          o4.x = strip(acc, o4.x, to); s2.x = strip(acc, s2.x, ts0); s2.y = strip(acc, s2.y, ts1);
          if (allok(acc)) break;
        }
        if (w == 0) STAMP(5);
        const float m = u2f(s2.x);
        float Mx = vmax(m, dpp<ROR4>(m));
        Mx = vmax(Mx, dpp<ROR8>(Mx)); Mx = max32(max16(Mx));
        const float f = ex2((m - Mx) * LOG2E);
        f32x4 o = f * asf(o4);
        float l = f * u2f(s2.y);
        o.x += dpp<ROR4>(o.x); o.y += dpp<ROR4>(o.y); o.z += dpp<ROR4>(o.z); o.w += dpp<ROR4>(o.w); l += dpp<ROR4>(l);
        l += dpp<ROR8>(l); l = sum32(sum16(l));
        const float ov = red4_345(o) * rcp(l);
        if ((lane & 12) == 0) *(LAS u16*)(L + L_OA + (bb * 64 + hh * 16 + (lane & 3) * 4 + comp4(lane)) * 2) = (u16)pk2(ov, ov);
      }
      BAR();
      if (w == 0) STAMP(6);
      {
        const v4u a0 = act_frag(L + L_OA, 64, 0, lane), a1 = act_frag(L + L_OA, 64, 32, lane);
        f32x4 d[8];
#pragma unroll
        for (int ct = 0; ct < 8; ++ct) d[ct] = mfma(a0, wf[ct], f32x4{0.f, 0.f, 0.f, 0.f});
#pragma unroll
        for (int ct = 0; ct < 8; ++ct) d[ct] = mfma(a1, wf[8 + ct], d[ct]);
        if (lane < 32) {
#pragma unroll
          for (int ct = 0; ct < 8; ++ct) st4(rs, e4a + ct * 128, (f2u(d[ct].x) & e4m) | e4t);
        }
        if (w == 0) STAMP(7);
      }
      if (w == 1) {
        {
          v4u a[4];
          for (int spin = 0; spin < SPIN_CAP; ++spin) {
            FENCE();
#pragma unroll
            for (int i = 0; i < 4; ++i) a[i] = ld16(rs, elo + (2 + i) * 65536);
            unsigned acc = 0u;
#pragma unroll
            for (int i = 0; i < 4; ++i) a[i].x = strip(acc, a[i].x, tel);
            if (allok(acc)) break;
          }
          const float sbv = rb(red4_345(asf(a[0]) + asf(a[1]))), cbv = rb(1.f + rb(red4_345(asf(a[2]) + asf(a[3]))));
          if ((lane & 8) == 0) { st4(rs, e5a + 8192, tg(sbv, t5)); st4(rs, e5a + 16384, tg(cbv, t5)); }
        }
      }
      if (w == 0) {
        float at, gav;
        {
          v4u a[8], e, e2;
          for (int spin = 0; spin < SPIN_CAP; ++spin) {
            FENCE();
#pragma unroll
            for (int i = 0; i < 8; ++i) a[i] = ld16(rs, e4o + i * 65536);
            e = ld16(rs, elo); e2 = ld16(rs, elo + 65536);
            unsigned acc = 0u;
#pragma unroll
            for (int i = 0; i < 8; ++i) a[i].x = strip(acc, a[i].x, t4);
            e.x = strip(acc, e.x, tel); e2.x = strip(acc, e2.x, tel);
            if (allok(acc)) break;
          }
          at = red4_345(((asf(a[0]) + asf(a[1])) + (asf(a[2]) + asf(a[3]))) + ((asf(a[4]) + asf(a[5])) + (asf(a[6]) + asf(a[7]))));
          gav = rb(red4_345(asf(e) + asf(e2)));
        }
        STAMP(10);
        const float x2 = rb(xv + rb(gav * rb(at)));
        if ((lane & 8) == 0) st4(rs, e5a, tg(x2, t5));
        STAMP(11);
        float gbv;
        {
          v4u e, e2;
          for (int spin = 0; spin < SPIN_CAP; ++spin) {
            FENCE();
            e = ld16(rs, elo + 6 * 65536); e2 = ld16(rs, elo + 7 * 65536);
            unsigned acc = 0u;
            e.x = strip(acc, e.x, tel); e2.x = strip(acc, e2.x, tel);
            if (allok(acc)) break;
          }
          gbv = rb(red4_345(asf(e) + asf(e2)));
        }
        float mo;
        {
          v4u a[7];
          for (int spin = 0; spin < SPIN_CAP; ++spin) {
            FENCE();
#pragma unroll
            for (int i = 0; i < 7; ++i) a[i] = ld16(rs, e6o + i * 65536);
            unsigned acc = 0u;
#pragma unroll
            for (int i = 0; i < 7; ++i) a[i].x = strip(acc, a[i].x, t6);
            if (allok(acc)) break;
          }
          mo = red4_345(((asf(a[0]) + asf(a[1])) + (asf(a[2]) + asf(a[3]))) + ((asf(a[4]) + asf(a[5])) + asf(a[6])));
        }
        const float ov = x2 + rb(gbv * rb(rb(mo) + b2v));
        if ((lane & 8) == 0) ((GAS u16*)p.out)[rbb * 1024 + col] = (u16)pk2(ov, ov);
        if (j == 0 && lane == 0) {
          const v2u e = *(const LAS v2u*)(L + L_EP);
          const u64 e1 = (((u64)e.y << 32) | e.x) + 1;
          st8(rs, WS_EPOCH, v2u{(unsigned)e1, (unsigned)(e1 >> 32)});
        }
        STAMP(12);
      }
    } else if (j < 120) {
      const int m = j - 64;
      const unsigned b1r = *(const GAS unsigned*)(p.b1 + m * 64 + (w & 3) * 16 + (lane & 7) * 2);
      const int bb = (tid >> 2) & 1, k0 = (tid >> 3) * 16 + (tid & 3) * 4;
      const int e5o = WS_E5 + tid * 16;
      v4u w1f[16], w2f[16];
      {
        const LAS char* img = L + w * 16384;
        const unsigned dst = lbase + w * 16384;
#define PACE() asm volatile("s_waitcnt vmcnt(%0)" ::"i"(MWIN - 1) : "memory")
        const int r8 = lane >> 3;
        const char* s1 = (const char*)(p.w1 + m * 64) + (size_t)(w * 128 + r8) * 7168;
        const int c0 = ((lane & 7) ^ (((r8 >> 1) & 1) << 1)) * 16;
        wait_until<D_M>(t0);
#pragma unroll
        for (int i = EARLY_W; i < 16; ++i) { if (i >= MWIN) PACE(); dma16(nsrc + (size_t)i * 4 * 2048 + row_src_off(i, lane), dst + i * 1024); }
        asm volatile("s_waitcnt vmcnt(%0)" ::"i"(MWIN < 8 ? MWIN - 1 : 8) : "memory");
#pragma unroll
        for (int ct = 0; ct < 8; ++ct) w2f[ct] = row_frag(img, 0, ct, lane);
#pragma unroll
        for (int ct = 0; ct < 8; ++ct) asm volatile("" : "+v"(w2f[ct]));
        asm volatile("s_waitcnt lgkmcnt(0)" ::: "memory");
#pragma unroll
        for (int i = 0; i < 8; ++i) { PACE(); dma16(s1 + (size_t)i * 8 * 7168 + (c0 ^ ((i & 1) * 64)), dst + i * 1024); }
        asm volatile("s_waitcnt vmcnt(%0)" ::"i"(MWIN < 8 ? MWIN - 1 : 8) : "memory");
#pragma unroll
        for (int ct = 0; ct < 8; ++ct) w2f[8 + ct] = row_frag(img, 1, ct, lane);
#pragma unroll
        for (int ct = 0; ct < 8; ++ct) asm volatile("" : "+v"(w2f[8 + ct]));
        asm volatile("s_waitcnt lgkmcnt(0)" ::: "memory");
#pragma unroll
        for (int i = 8; i < 16; ++i) { PACE(); dma16(s1 + (size_t)i * 8 * 7168 + (c0 ^ ((i & 1) * 64)), dst + i * 1024); }
        VMCNT(0);
#pragma unroll
        for (int ks = 0; ks < 4; ++ks)
#pragma unroll
          for (int ct = 0; ct < 4; ++ct) w1f[ks * 4 + ct] = col_frag(img, ks, ct, lane);
#pragma unroll
        for (int i = 0; i < 16; ++i) asm volatile("" : "+v"(w1f[i]));
        if (w == 0) STAMP(12);
      }
      const int e6a = WS_E6 + (m * 64 + 8 * w) * 128 + (g & 1) * 64 + c * 4;
      const unsigned e6m = (c & 3) == 0 ? 0xFFFFFF00u : 0xFFFFFFFFu, e6t = (c & 3) == 0 ? (tagb(T, e6a) & 0xFFu) : 0u;
      const unsigned t5 = tagb(T, e5o);
      LAS float* lns = (LAS float*)(L + L_LN);
      LAS float* lnw = lns + (bb * 2 + (lane >> 4)) * 8 + w;
      const LAS f32x4* lnr = (const LAS f32x4*)(lns + bb * 16);
      f32x2 v0, v1;
      {
        v4u xv;
        for (int spin = 0; spin < SPIN_CAP; ++spin) {
          FENCE();
          xv = ld16(rs, e5o);
          if (allok(chk(chk(chk(chk(0u, xv.x, t5), xv.y, t5 >> 8), xv.z, t5 >> 16), xv.w, t5 >> 24))) break;
        }
        v0 = f32x2{bfhi(xv.x), bfhi(xv.y)}; v1 = f32x2{bfhi(xv.z), bfhi(xv.w)};
      }
      v4u sbr = ld16(rs, e5o + 8192), cbr = ld16(rs, e5o + 16384);
      if (w == 0) STAMP(5);
      float mb, rd;
      auto stats = [&](int stage) {
        const f32x2 s2 = v0 + v1, q2 = v0 * v0 + v1 * v1;
        float s = s2.x + s2.y, q = q2.x + q2.y;
        s += dpp<XOR1>(s); q += dpp<XOR1>(q);
        s += dpp<XOR2>(s); q += dpp<XOR2>(q);
        s += dpp<ROR8>(s); q += dpp<ROR8>(q);
        const float t = sum32(fold16(s, q));
        if ((lane & ~20) == 0) lnw[stage * 32] = t;
        BAR();
        const f32x4 a = lnr[stage * 8] + lnr[stage * 8 + 1], e = lnr[stage * 8 + 2] + lnr[stage * 8 + 3];
        const float mean = ((a.x + a.y) + (a.z + a.w)) * (1.f / 1024.f);
        const float var = fmaf(-mean, mean, ((e.x + e.y) + (e.z + e.w)) * (1.f / 1024.f));
        mb = rb(mean); rd = ln_rden(fmaxf(var, 0.f));
      };
      stats(0);
      if (w == 0) STAMP(13);
      f32x2 sb0, sb1, cb0, cb1;
      {
        for (int spin = 0; spin < SPIN_CAP; ++spin) {
          if (allok(chk(chk(chk(chk(chk(chk(chk(chk(0u, sbr.x, t5), sbr.y, t5 >> 8), sbr.z, t5 >> 16), sbr.w, t5 >> 24), cbr.x, t5), cbr.y, t5 >> 8), cbr.z, t5 >> 16), cbr.w, t5 >> 24))) break;
          FENCE();
          sbr = ld16(rs, e5o + 8192); cbr = ld16(rs, e5o + 16384);
        }
        sb0 = f32x2{bfhi(sbr.x), bfhi(sbr.y)}; sb1 = f32x2{bfhi(sbr.z), bfhi(sbr.w)};
        cb0 = f32x2{bfhi(cbr.x), bfhi(cbr.y)}; cb1 = f32x2{bfhi(cbr.z), bfhi(cbr.w)};
      }
      v0 = rb2(rb2(rb2(rb2(v0 - mb) * rd) * cb0) + sb0);
      v1 = rb2(rb2(rb2(rb2(v1 - mb) * rd) * cb1) + sb1);
      stats(1);
      if (w == 0) STAMP(14);
      *(LAS v2u*)(L + L_ACT + (bb * 1024 + k0) * 2) = v2u{pk2(rb2(v0 - mb) * rd), pk2(rb2(v1 - mb) * rd)};
      asm volatile("s_waitcnt lgkmcnt(0)" ::: "memory");
      if (w == 0) STAMP(6);
      {
        v4u a[4];
#pragma unroll
        for (int ks = 0; ks < 4; ++ks) a[ks] = act_frag(L + L_ACT, 1024, w * 128 + ks * 32, lane);
        f32x4 d[4];
#pragma unroll
        for (int ct = 0; ct < 4; ++ct) d[ct] = mfma(a[0], w1f[ct], f32x4{0.f, 0.f, 0.f, 0.f});
#pragma unroll
        for (int ks = 1; ks < 4; ++ks)
#pragma unroll
          for (int ct = 0; ct < 4; ++ct) d[ct] = mfma(a[ks], w1f[ks * 4 + ct], d[ct]);
        col_red_store(red, w, lane, d);
      }
      BAR();
      if (w == 0) STAMP(15);
      {
        const f32x2 z = rb2(rb2(col_red_sum(red, w, lane)) + up2(b1r));
        const f32x2 u = (z + 0.044715f * z * z * z) * (-2.f * 0.7978845608028654f * LOG2E);
        const float g0 = z.x * rcp(1.f + ex2(u.x)), g1 = z.y * rcp(1.f + ex2(u.y));
        if (lane < 8) *(LAS unsigned*)(L + L_OA + ((w >> 2) * 64 + (w & 3) * 16 + lane * 2) * 2) = pk2(g0, g1);
      }
      BAR();
      if (w == 0) STAMP(7);
      {
        const v4u a0 = act_frag(L + L_OA, 64, 0, lane), a1 = act_frag(L + L_OA, 64, 32, lane);
        f32x4 d[8];
#pragma unroll
        for (int ct = 0; ct < 8; ++ct) d[ct] = mfma(a0, w2f[ct], f32x4{0.f, 0.f, 0.f, 0.f});
#pragma unroll
        for (int ct = 0; ct < 8; ++ct) d[ct] = mfma(a1, w2f[8 + ct], d[ct]);
        if (lane < 32) {
#pragma unroll
          for (int ct = 0; ct < 8; ++ct) st4(rs, e6a + ct * 128, (f2u(d[ct].x) & e6m) | e6t);
        }
        if (w == 0) STAMP(8);
      }
    }
  }
  if (w == 0) STAMP_FLUSH();
}

}

extern "C" void kernel_launch(void* const* d_in, const int* in_sizes, int n_in, void* d_out, int out_size, void* d_ws, size_t ws_size,
                              hipStream_t stream) {
  Params p;
  p.x = (const u16*)d_in[0]; p.kc = (const u16*)d_in[1]; p.vc = (const u16*)d_in[2]; p.wq = (const u16*)d_in[3]; p.wo = (const u16*)d_in[4];
  p.w1 = (const u16*)d_in[6]; p.b1 = (const u16*)d_in[7]; p.w2 = (const u16*)d_in[8]; p.b2 = (const u16*)d_in[9];
  p.c = (const u16*)d_in[10]; p.wmod = (const u16*)d_in[11];
  p.out = (u16*)d_out; p.ws = (unsigned char*)d_ws;
  (void)hipFuncSetAttribute((const void*)fused_kernel, hipFuncAttributeMaxDynamicSharedMemorySize, LDS_BYTES);
  fused_kernel<<<256, NTHR, LDS_BYTES, stream>>>(p);
}
```

```cpp
#define THR 2050.f
#include <hip/hip_runtime.h>
#include <hip/hip_bf16.h>
#include <stdint.h>

namespace {

typedef unsigned short u16;
typedef unsigned long long u64;
typedef unsigned v4u __attribute__((ext_vector_type(4)));
typedef unsigned v2u __attribute__((ext_vector_type(2)));
typedef float f32x4 __attribute__((ext_vector_type(4)));
typedef float f32x2 __attribute__((ext_vector_type(2)));
typedef __bf16 bf16x8 __attribute__((ext_vector_type(8)));
typedef __bf16 bf16x2 __attribute__((ext_vector_type(2)));
typedef short s16x4 __attribute__((ext_vector_type(4)));
#define GAS __attribute__((address_space(1)))
#define LAS __attribute__((address_space(3)))

constexpr int NTHR = 512;
constexpr u64 TAGK = 0x9E3779B97F4A7C15ull, TAGC = 0x0E56CD77ull;
constexpr int SPIN_CAP = 1 << 16;

constexpr int WS_EPOCH = 0;
constexpr int WS_E1 = 8192;
constexpr int WS_E2 = WS_E1 + 65536;
constexpr int WS_E3 = WS_E2 + 131072;
constexpr int WS_E3S = WS_E3 + 524288;
constexpr int WS_E4 = WS_E3S + 16384;
constexpr int WS_E5 = WS_E4 + 524288;
constexpr int WS_EL = WS_E5 + 24576;
constexpr int WS_E6 = WS_EL + 524288;
constexpr int WS_END = WS_E6 + 458752;
constexpr int WS_SIZE = WS_END + 4096;

constexpr int L_K = 0, L_V = 65536;
constexpr int L_XC = 98304;
constexpr int L_ACT = 131072;
constexpr int L_RED = L_ACT + 4096;
constexpr int L_QS = L_RED + 4096;
constexpr int L_PA = L_QS + 2304;
constexpr int L_ML = L_PA + 8192;
constexpr int L_OA = L_ML + 512;
constexpr int L_LA = L_OA + 256;
constexpr int L_LN = L_LA + 2048;
constexpr int L_PAIR = L_LN + 256;
constexpr int L_EP = L_PAIR + 16;
constexpr int L_PF = L_EP + 1024;
constexpr int LDS_BYTES = L_PF + 1024;

#define D_Y 30
#define D_K 100
#define D_PE1 150
#define D_WO 0
#define D_M 0
#define LATE_W 0
#define LATE_A 0
#define EARLY_W 0
#define LATE_POS 1
#define PRETOUCH 0
#define XMAP 0
#define D_LATE 0
#define SP1 0
#define SP2 0
#define SP3 0
#define ST_SC 0
#define ST_GB 0
#define MWIN 16
#define PF_SLICES 11

struct Params {
  const u16 *x, *kc, *vc, *wq, *wo, *w1, *b1, *w2, *b2, *c, *wmod;
  u16* out;
  unsigned char* ws;
};

__device__ __forceinline__ u64 rt() { return __builtin_amdgcn_s_memtime(); }
template <int D> __device__ __forceinline__ void wait_until(u64 t0) { if (D > 0) { while ((long long)(rt() - t0) < (long long)D * 21) {} } }
__device__ __forceinline__ float u2f(unsigned u) { return __builtin_bit_cast(float, u); }
__device__ __forceinline__ unsigned f2u(float f) { return __builtin_bit_cast(unsigned, f); }
__device__ __forceinline__ float bflo(unsigned b) { return u2f(b << 16); }
__device__ __forceinline__ float bfhi(unsigned b) { return u2f(b & 0xFFFF0000u); }
__device__ __forceinline__ unsigned pk2(float lo, float hi) { f32x2 v = {lo, hi}; return __builtin_bit_cast(unsigned, __builtin_convertvector(v, bf16x2)); }
__device__ __forceinline__ unsigned pk2(f32x2 v) { return __builtin_bit_cast(unsigned, __builtin_convertvector(v, bf16x2)); }
__device__ __forceinline__ f32x2 up2(unsigned b) { return f32x2{bflo(b), bfhi(b)}; }
__device__ __forceinline__ f32x2 rb2(f32x2 v) { return up2(pk2(v)); }
__device__ __forceinline__ float rb(float f) { return bfhi(pk2(f, f)); }
__device__ __forceinline__ unsigned tagb(unsigned T, int A) { return __builtin_amdgcn_alignbit(T, T, 8 * (((A >> 2) + (A >> 4)) & 3)); }
__device__ __forceinline__ unsigned tg(float f, unsigned tb) { return (f2u(f) & 0xFFFFFF00u) | (tb & 0xFFu); }
__device__ __forceinline__ unsigned chk(unsigned acc, unsigned w, unsigned tb) { return acc | (w ^ tb); }
__device__ __forceinline__ unsigned tag2(unsigned T, int A) { return T >> (2 * ((A >> 2) & 15)); }
__device__ __forceinline__ unsigned tg2(float f, unsigned t) { return (f2u(f) & ~3u) | (t & 3u); }
__device__ __forceinline__ bool allok2(unsigned acc) { return __builtin_amdgcn_ballot_w64((acc & 3u) != 0u) == 0ull; }
__device__ __forceinline__ bool allok(unsigned acc) { return __builtin_amdgcn_ballot_w64((acc & 0xFFu) != 0u) == 0ull; }
__device__ __forceinline__ float vmax(float a, float b) { float r; asm("v_max_f32 %0, %1, %2" : "=v"(r) : "v"(a), "v"(b)); return r; }
__device__ __forceinline__ float vmax3(float a, float b, float c) { float r; asm("v_max3_f32 %0, %1, %2, %3" : "=v"(r) : "v"(a), "v"(b), "v"(c)); return r; }
__device__ __forceinline__ void keep(unsigned v) { asm volatile("" ::"v"(v)); }
__device__ __forceinline__ float rcp(float x) { return __builtin_amdgcn_rcpf(x); }
__device__ __forceinline__ float ex2(float x) { return __builtin_amdgcn_exp2f(x); }
constexpr float LOG2E = 1.4426950408889634f;
__device__ __forceinline__ f32x4 mfma(v4u a, v4u b, f32x4 c) {
  return __builtin_amdgcn_mfma_f32_16x16x32_bf16(__builtin_bit_cast(bf16x8, a), __builtin_bit_cast(bf16x8, b), c, 0, 0, 0);
}
#define BAR() asm volatile("s_waitcnt lgkmcnt(0)\n\ts_barrier" ::: "memory")
#define VMCNT(n) asm volatile("s_waitcnt vmcnt(" #n ")" ::: "memory")
#define SB() __builtin_amdgcn_sched_barrier(0)
#define FENCE() asm volatile("" ::: "memory")
constexpr int SC1 = 16;
typedef __amdgpu_buffer_rsrc_t rsrc_t;
__device__ __forceinline__ v4u ld16(rsrc_t rs, int off) { return __builtin_amdgcn_raw_buffer_load_b128(rs, off, 0, SC1); }
__device__ __forceinline__ v2u ld8(rsrc_t rs, int off) { return __builtin_amdgcn_raw_buffer_load_b64(rs, off, 0, SC1); }
__device__ __forceinline__ unsigned ld4(rsrc_t rs, int off) { return __builtin_amdgcn_raw_buffer_load_b32(rs, off, 0, SC1); }
__device__ __forceinline__ void st16(rsrc_t rs, int off, v4u v) { __builtin_amdgcn_raw_buffer_store_b128(v, rs, off, 0, SC1); }
__device__ __forceinline__ void st8(rsrc_t rs, int off, v2u v) { __builtin_amdgcn_raw_buffer_store_b64(v, rs, off, 0, SC1); }
__device__ __forceinline__ void st4(rsrc_t rs, int off, unsigned v) { __builtin_amdgcn_raw_buffer_store_b32(v, rs, off, 0, SC1); }
__device__ __forceinline__ v4u ldnt(const void* p) { return __builtin_nontemporal_load((const GAS v4u*)p); }
__device__ __forceinline__ f32x4 asf(v4u v) { return __builtin_bit_cast(f32x4, v); }
__device__ __forceinline__ f32x2 asf2(v2u v) { return __builtin_bit_cast(f32x2, v); }
__device__ __forceinline__ v4u tg1(f32x4 v, unsigned T, int A) { return v4u{tg(v.x, tagb(T, A)), f2u(v.y), f2u(v.z), f2u(v.w)}; }
__device__ __forceinline__ unsigned strip(unsigned& acc, unsigned w, unsigned tb8) { const unsigned x = w ^ tb8; acc |= x; return x; }
__device__ __forceinline__ v4u tg4(f32x4 v, unsigned T, int A) { const unsigned t = tagb(T, A); return v4u{tg(v.x, t), tg(v.y, t >> 8), tg(v.z, t >> 16), tg(v.w, t >> 24)}; }

template <int CTRL> __device__ __forceinline__ float dpp(float x) {
  return u2f((unsigned)__builtin_amdgcn_mov_dpp((int)f2u(x), CTRL, 0xf, 0xf, true));
}
constexpr int XOR1 = 0xB1, XOR2 = 0x4E, ROR8 = 0x128, ROR4 = 0x124;
__device__ __forceinline__ float fold16(float a, float b) { auto s = __builtin_amdgcn_permlane16_swap(f2u(a), f2u(b), false, false); const unsigned p = s[0], q = s[1]; return u2f(p) + u2f(q); }
__device__ __forceinline__ float fold32(float a, float b) { auto s = __builtin_amdgcn_permlane32_swap(f2u(a), f2u(b), false, false); const unsigned p = s[0], q = s[1]; return u2f(p) + u2f(q); }
__device__ __forceinline__ float sum16(float x) { return fold16(x, x); }
__device__ __forceinline__ float sum32(float x) { return fold32(x, x); }
__device__ __forceinline__ float max16(float x) { auto s = __builtin_amdgcn_permlane16_swap(f2u(x), f2u(x), false, false); const unsigned p = s[0], q = s[1]; return vmax(u2f(p), u2f(q)); }
__device__ __forceinline__ float max32(float x) { auto s = __builtin_amdgcn_permlane32_swap(f2u(x), f2u(x), false, false); const unsigned p = s[0], q = s[1]; return vmax(u2f(p), u2f(q)); }
__device__ __forceinline__ float red4_345(f32x4 v) {
  const float a = v.x + dpp<ROR8>(v.x), b = v.y + dpp<ROR8>(v.y), c = v.z + dpp<ROR8>(v.z), d = v.w + dpp<ROR8>(v.w);
  return fold32(fold16(a, b), fold16(c, d));
}
__device__ __forceinline__ int comp4(int lane) { return lane >> 4; }

#define STAMP(i) do {} while (0)
#define STAMP_FLUSH() do {} while (0)

__device__ __forceinline__ void dma16(const void* gsrc, unsigned ldsaddr) {
  unsigned keep;
  asm volatile("s_mov_b32 %0, m0\n\ts_mov_b32 m0, %2\n\ts_nop 0\n\tglobal_load_lds_dwordx4 %1, off nt\n\ts_mov_b32 m0, %0"
               : "=&s"(keep) : "v"(gsrc), "s"(ldsaddr) : "memory");
}
__device__ __forceinline__ void dma16_keep(const void* gsrc, unsigned ldsaddr) {
  unsigned keep;
  asm volatile("s_mov_b32 %0, m0\n\ts_mov_b32 m0, %2\n\ts_nop 0\n\tglobal_load_lds_dwordx4 %1, off\n\ts_mov_b32 m0, %0"
               : "=&s"(keep) : "v"(gsrc), "s"(ldsaddr) : "memory");
}
template <int NI>
__device__ __forceinline__ void dma_col(const u16* base, int row0, int stride_b, unsigned lds, int lane) {
  const int r = lane >> 3;
  const char* src = (const char*)base + (size_t)(row0 + r) * stride_b;
  const int c0 = ((lane & 7) ^ (((r >> 1) & 1) << 1)) * 16, c1 = c0 ^ 64;
#pragma unroll
  for (int i = 0; i < NI; ++i) dma16(src + (size_t)i * 8 * stride_b + ((i & 1) ? c1 : c0), lds + i * 1024);
}
__device__ __forceinline__ v4u tr_frag(const LAS char* p0, const LAS char* p1) {
  const v2u u0 = __builtin_bit_cast(v2u, __builtin_amdgcn_ds_read_tr16_b64_v4i16((LAS s16x4*)p0));
  const v2u u1 = __builtin_bit_cast(v2u, __builtin_amdgcn_ds_read_tr16_b64_v4i16((LAS s16x4*)p1));
  return v4u{u0.x, u0.y, u1.x, u1.y};
}
__device__ __forceinline__ v4u col_frag(const LAS char* img, int ks, int ct, int lane) {
  const int g = lane >> 4, q = (lane & 15) >> 2, pp = lane & 3;
  const int sw = ((q >> 1) & 1) | ((g & 1) << 1);
  const LAS char* p = img + (ks * 32 + 8 * g + q) * 128 + ((ct ^ sw) * 32) + pp * 8;
  return tr_frag(p, p + 512);
}
__device__ __forceinline__ int row_src_off(int i, int lane) { return ((lane & 15) ^ (2 * (lane >> 4)) ^ (((i >> 1) & 1) << 3)) * 16; }
__device__ __forceinline__ size_t next_row(int j, int i) { return j < 64 ? (size_t)((i >> 2) * 128 + (i & 3) * 4) : (size_t)(i * 4); }
__device__ __forceinline__ v4u row_frag(const LAS char* img, int ks, int ct, int lane) {
  const int g = lane >> 4, q = (lane & 15) >> 2, pp = lane & 3;
  const LAS char* p = img + (ks * 32 + 8 * g + q) * 256 + ((ct ^ (q | ((g & 1) << 2))) * 32) + pp * 8;
  return tr_frag(p, p + 1024);
}
__device__ __forceinline__ v4u act_frag(const LAS char* act, int kstr, int k0, int lane) {
  return *(const LAS v4u*)(act + (((lane >> 2) & 1) * kstr + k0 + (lane >> 4) * 8) * 2);
}
__device__ __forceinline__ void col_red_store(LAS float* red, int w, int lane, const f32x4 (&d)[4]) {
  if (lane < 32) {
    LAS float* p = red + w * 128 + (lane >> 4) * 64 + (lane & 15);
    p[0] = d[0].x; p[16] = d[1].x; p[32] = d[2].x; p[48] = d[3].x;
  }
}
__device__ __forceinline__ f32x4 col_red_sum_all(const LAS float* red, int lane) {
  const LAS f32x4* pr = (const LAS f32x4*)(red + (lane >> 5) * 4 * 128 + (lane & 31) * 4);
  f32x4 s = (pr[0] + pr[32]) + (pr[64] + pr[96]);
  const float a = fold32(s.x, s.y), b = fold32(s.z, s.w);
  auto sa = __builtin_amdgcn_permlane32_swap(f2u(a), f2u(a), false, false), sb = __builtin_amdgcn_permlane32_swap(f2u(b), f2u(b), false, false);
  const unsigned x = sa[0], y = sa[1], z = sb[0], ww = sb[1];
  return f32x4{u2f(x), u2f(y), u2f(z), u2f(ww)};
}
__device__ __forceinline__ f32x2 col_red_sum(const LAS float* red, int w, int lane) {
  f32x2 s = *(const LAS f32x2*)(red + (lane >> 3) * 128 + w * 16 + (lane & 7) * 2);
  s.x += dpp<ROR8>(s.x); s.y += dpp<ROR8>(s.y);
  s.x = sum16(s.x); s.y = sum16(s.y);
  s.x = sum32(s.x); s.y = sum32(s.y);
  return s;
}
__device__ __forceinline__ float silu_rb(float v) { return rb(v * rcp(1.f + ex2(-LOG2E * v))); }
__device__ __forceinline__ float ln_rden(float var) { return rcp(rb(__builtin_amdgcn_sqrtf(rb(rb(var) + 1e-5f)))); }
__device__ __forceinline__ unsigned make_tag(const LAS char* L) {
  const v2u e = *(const LAS v2u*)(L + L_EP);
  const u64 epoch = ((u64)__builtin_amdgcn_readfirstlane(e.y) << 32) | (unsigned)__builtin_amdgcn_readfirstlane(e.x);
  return (unsigned)(((epoch + TAGC) * TAGK) >> 32);
}

__global__ void __launch_bounds__(NTHR, 2) fused_kernel(Params p) {
  extern __shared__ __attribute__((aligned(16))) char smem[];
  LAS char* L = (LAS char*)smem;
  const u64 t0 = rt();
  const u64 real00 = __builtin_amdgcn_s_memrealtime();
  const int tid = threadIdx.x, lane = tid & 63, w = __builtin_amdgcn_readfirstlane(tid >> 6), bid = blockIdx.x;
  const unsigned lbase = (unsigned)(size_t)L;
  const bool isX = bid < 128;
  const int jj = bid & 127;
  {
    const int r = isX ? lane >> 3 : lane >> 4;
    const int row0 = (jj & 3) * 256 + w * 32, stride = isX ? 12288 : 8192;
    const char* base = isX ? (const char*)(p.wmod + (jj >> 2) * 64) : (const char*)(p.wq + (jj >> 2) * 128);
    const char* src = base + (size_t)(row0 + r) * stride;
    const int c0 = isX ? ((lane & 7) ^ (((r >> 1) & 1) << 1)) * 16 : ((lane & 15) ^ (2 * r)) * 16;
    const unsigned dst = lbase + (isX ? L_V + w * 4096 : w * 8192);
    if (w == 0) dma16_keep(p.ws + WS_EPOCH + lane * 16, lbase + L_EP);
    if (PRETOUCH && w == 1) {
      const int ln = min(bid * 64 + lane, (WS_END - WS_E1) / 128 - 1);
      dma16_keep(p.ws + WS_E1 + (size_t)ln * 128, lbase + L_PF);
    }
    if (w >= 6) {
      u64 pc;
      asm volatile("s_getpc_b64 %0" : "=s"(pc));
      const int sl = (bid >> 3) & 15;
      if (sl < PF_SLICES) dma16_keep((const char*)(pc & ~255ull) + sl * 2048 + (w - 6) * 1024 + lane * 16, lbase + L_PF);
    }
    if (isX) {
      dma16(p.c + ((lane >> 2) & 1) * 1024 + row0 + (lane & 3) * 8, lbase + L_XC + w * 2048);
      dma16(p.c + ((lane >> 2) & 1) * 1024 + (jj & 15) * 64 + (w & 1) * 32 + (lane & 3) * 8, lbase + L_XC + w * 2048 + 1024);
#pragma unroll
      for (int i = 0; i < 4; ++i) dma16(src + (size_t)i * 8 * stride + (c0 ^ ((i & 1) * 64)), dst + i * 1024);
    } else {
#pragma unroll
      for (int i = 0; i < 4; ++i) dma16(p.x + i * 512 + lane * 8, lbase + L_XC + i * 1024);
      wait_until<D_Y>(t0);
#pragma unroll
      for (int i = 0; i < 8; ++i) dma16(src + (size_t)i * 4 * stride + (c0 ^ (((i >> 1) & 1) * 128)), dst + i * 1024);
    }
  }
  const rsrc_t rs = __builtin_amdgcn_make_buffer_rsrc((void*)p.ws, 0, WS_SIZE, 0x00020000);
  LAS float* red = (LAS float*)(L + L_RED);
  const int g = lane >> 4, c = lane & 15;
  if (w == 0 && lane == 0) { const u64 r0 = real00; st8(rs, WS_END + bid * 8, v2u{(unsigned)r0, (unsigned)(r0 >> 32)}); }

  if (__builtin_expect(isX, 1)) {
    const int j = jj, kq = j & 3, cg = j >> 2;
    const int b = j >> 6, hkv = (j >> 4) & 3, sp = j & 15;
    LAS char* act = L + L_ACT + w * 128;
    LAS char* lact = L + L_LA + w * 256;
    VMCNT(4);
    unsigned T = 0;
    {
      const unsigned craw = *(const LAS u16*)(L + L_XC + w * 2048 + lane * 2);
      const unsigned c2 = *(const LAS u16*)(L + L_XC + w * 2048 + 1024 + lane * 2);
      const float s = silu_rb(bflo(craw)), s2 = silu_rb(bflo(c2));
      *(LAS u16*)(act + lane * 2) = (u16)pk2(s, s);
      *(LAS u16*)(lact + lane * 2) = (u16)pk2(s2, s2);
    }
    if (tid < 4) ((volatile LAS unsigned*)(L + L_PAIR))[tid] = 0u;
    if (w == 0) T = make_tag(L);
    asm volatile("s_waitcnt lgkmcnt(0)" ::: "memory");
    const v4u a1 = act_frag(act, 32, 0, lane);
    const int e1a = WS_E1 + ((kq * 2 + ((lane >> 4) & 1)) * 2048 + cg * 64 + (lane & 15) * 4) * 4;
    const unsigned tb1 = tag2(T, e1a);
    VMCNT(0);
    {
      f32x4 d[4];
#pragma unroll
      for (int ct = 0; ct < 4; ++ct) d[ct] = mfma(a1, col_frag(L + L_V + w * 4096, 0, ct, lane), f32x4{0.f, 0.f, 0.f, 0.f});
      col_red_store(red, w, lane, d);
    }
    if (w == 0) STAMP(1);
    BAR();
    if (w == 0) {
      const f32x4 s = col_red_sum_all(red, lane);
      if (lane < 32) st16(rs, e1a, v4u{tg2(s.x, tb1), tg2(s.y, tb1 >> 2), tg2(s.z, tb1 >> 4), tg2(s.w, tb1 >> 6)});
      STAMP(2);
    } else T = make_tag(L);
    BAR();
    {
      const size_t kvoff = ((size_t)(b * 4 + hkv) * 32768 + (32768 - 4096) + sp * 256 + w * 32) * 256;
      const char* ksrc = (const char*)p.kc + kvoff + g * 256;
      const char* vsrc = (const char*)p.vc + kvoff + g * 256;
      wait_until<D_K>(t0);
#pragma unroll
      for (int i = 0; i < 8; ++i) dma16(ksrc + i * 1024 + ((c ^ ((i * 4 + g) & 15)) * 16), lbase + L_K + w * 8192 + i * 1024);
#pragma unroll
      for (int i = 0; i < 8; ++i) dma16(vsrc + i * 1024 + ((c ^ (((i * 4 + g) & 7) << 1)) * 16), lbase + L_V + w * 8192 + i * 1024);
    }
    const LAS char* qp = L + L_QS + (c & 7) * 272 + g * 16;
    LAS float* mlw = (LAS float*)(L + L_ML) + (c & 7) * 8;
    const int e3oa = WS_E3 + (((b * 4 + hkv) * 4) * 16 + sp) * 1024 + w * 128 + (g & 1) * 64 + c * 4;
    const unsigned e3m = (c & 3) == 0 ? 0xFFFFFF00u : 0xFFFFFFFFu, e3t = (c & 3) == 0 ? (tagb(T, e3oa) & 0xFFu) : 0u;
    const int e3sa = WS_E3S + ((b * 4 + hkv) * 16 + sp) * 128 + c * 8;
    const unsigned tb3s = tagb(T, e3sa);
    const int qa = WS_E2 + ((g * 2 + b) * 4096 + (hkv * 8 + w) * 128 + c * 8) * 4;
    const unsigned tq = tag2(T, qa);
    LAS char* qd = L + L_QS + w * 272 + (c * 8 + g) * 2;
    const char* lsrc = (const char*)(p.wmod + 2048 + (w >> 1) * 1024 + (j >> 4) * 128) + (size_t)((j & 15) * 64 + (w & 1) * 32 + g) * 12288;
    auto late_dma = [&](int lo, int hi) {
#pragma unroll
      for (int i = 0; i < 8; ++i) if (i >= lo && i < hi) dma16(lsrc + (size_t)i * 4 * 12288 + row_src_off(i, lane), lbase + L_K + w * 8192 + i * 1024);
    };
    VMCNT(8);
    if (w == 0) STAMP(5);
    v4u kf[8];
#pragma unroll
    for (int Tt = 0; Tt < 2; ++Tt)
#pragma unroll
      for (int s = 0; s < 4; ++s) kf[Tt * 4 + s] = *(const LAS v4u*)(L + L_K + w * 8192 + (Tt * 16 + c) * 256 + (((s * 4 + g) ^ c) * 16));
#pragma unroll
    for (int i = 0; i < 8; ++i) asm volatile("" : "+v"(kf[i]));
    if (w < LATE_W) {
      asm volatile("s_waitcnt lgkmcnt(0)" ::: "memory");
      late_dma(0, 8);
      VMCNT(8);
    } else VMCNT(0);
    if (w == 0) STAMP(6);
    BAR();
    v4u vf[8];
    {
      const int q = c >> 2, pp = c & 3, r0 = 4 * g + q;
      const LAS char* vb = L + L_V + r0 * 256 + ((w ^ (r0 & 7)) * 32) + pp * 8;
#pragma unroll
      for (int ks = 0; ks < 8; ++ks) vf[ks] = tr_frag(vb + ks * 8192, vb + ks * 8192 + 4096);
    }
#pragma unroll
    for (int i = 0; i < 8; ++i) asm volatile("" : "+v"(vf[i]));
    {
      v4u v0, v1;
      for (int spin = 0; spin < SPIN_CAP; ++spin) {
        FENCE();
        v0 = ld16(rs, qa); v1 = ld16(rs, qa + 16);
        unsigned acc = 0u;
        v0.x = strip(acc, v0.x, tq & 3u); v0.y = strip(acc, v0.y, (tq >> 2) & 3u); v0.z = strip(acc, v0.z, (tq >> 4) & 3u); v0.w = strip(acc, v0.w, (tq >> 6) & 3u);
        v1.x = strip(acc, v1.x, (tq >> 8) & 3u); v1.y = strip(acc, v1.y, (tq >> 10) & 3u); v1.z = strip(acc, v1.z, (tq >> 12) & 3u); v1.w = strip(acc, v1.w, (tq >> 14) & 3u);
        if (allok2(acc)) break;
      }
      const float q0 = fold32(fold16(u2f(v0.x), u2f(v0.y)), fold16(u2f(v0.z), u2f(v0.w)));
      const float q1 = fold32(fold16(u2f(v1.x), u2f(v1.y)), fold16(u2f(v1.z), u2f(v1.w)));
      const unsigned pq = pk2(q0, q1);
      *(LAS u16*)qd = (u16)pq; *(LAS u16*)(qd + 8) = (u16)(pq >> 16);
      if (w == 0) STAMP(3);
    }
    if (w >= LATE_W) late_dma(0, SP1);
    BAR();
    if (w == 0) STAMP(4);
    f32x4 sc[2];
    {
      v4u qf[4];
#pragma unroll
      for (int s = 0; s < 4; ++s) qf[s] = *(const LAS v4u*)(qp + s * 64);
      SB();
      f32x4 d0 = mfma(kf[0], qf[0], f32x4{0.f, 0.f, 0.f, 0.f}), d1 = mfma(kf[4], qf[0], f32x4{0.f, 0.f, 0.f, 0.f});
#pragma unroll
      for (int s = 1; s < 4; ++s) { d0 = mfma(kf[s], qf[s], d0); d1 = mfma(kf[4 + s], qf[s], d1); }
      sc[0] = d0; sc[1] = d1;
    }
    {
      const f32x2 sca = {0.08838834764831845f, 0.08838834764831845f};
#pragma unroll
      for (int Tt = 0; Tt < 2; ++Tt) {
        const f32x2 lo = rb2(rb2(f32x2{sc[Tt].x, sc[Tt].y}) * sca), hi = rb2(rb2(f32x2{sc[Tt].z, sc[Tt].w}) * sca);
        sc[Tt] = f32x4{lo.x, lo.y, hi.x, hi.y};
      }
      float mx = vmax(vmax3(vmax3(sc[0].x, sc[0].y, sc[0].z), sc[0].w, sc[1].x), vmax3(sc[1].y, sc[1].z, sc[1].w));
      mx = max32(max16(mx));
      if (lane < 8) mlw[w] = mx;
    }
    if (w >= LATE_W) late_dma(SP1, SP2);
    if (w == 0) STAMP(15);
    BAR();
    float M;
    {
      const f32x4 m0 = *(const LAS f32x4*)mlw, m1 = *(const LAS f32x4*)(mlw + 4);
      M = vmax(vmax3(vmax3(m0.x, m0.y, m0.z), m0.w, m1.x), vmax3(m1.y, m1.z, m1.w));
      const float nm = -M * LOG2E;
#pragma unroll
      for (int Tt = 0; Tt < 2; ++Tt)
#pragma unroll
        for (int r = 0; r < 4; ++r) sc[Tt][r] = ex2(fmaf(sc[Tt][r], LOG2E, nm));
      *(LAS v4u*)(L + L_PA + w * 1024 + lane * 16) = v4u{pk2(sc[0].x, sc[0].y), pk2(sc[0].z, sc[0].w), pk2(sc[1].x, sc[1].y), pk2(sc[1].z, sc[1].w)};
      const f32x4 t4 = sc[0] + sc[1];
      const float ls = sum32(sum16((t4.x + t4.y) + (t4.z + t4.w)));
      if (lane < 8) mlw[64 + w] = ls;
    }
    if (w >= LATE_W) late_dma(SP2, SP3);
    BAR();
    if (w == 0) STAMP(14);
    {
      v4u pa[8];
#pragma unroll
      for (int ks = 0; ks < 8; ++ks) pa[ks] = *(const LAS v4u*)(L + L_PA + ks * 1024 + lane * 16);
      SB();
      f32x4 o0 = mfma(pa[0], vf[0], f32x4{0.f, 0.f, 0.f, 0.f}), o1 = mfma(pa[1], vf[1], f32x4{0.f, 0.f, 0.f, 0.f});
#pragma unroll
      for (int ks = 2; ks < 8; ks += 2) { o0 = mfma(pa[ks], vf[ks], o0); o1 = mfma(pa[ks + 1], vf[ks + 1], o1); }
      const f32x4 o = o0 + o1;
      if (lane < 32) {
#pragma unroll
        for (int r = 0; r < 4; ++r) st4(rs, e3oa + r * 16384, (f2u(o[r]) & e3m) | e3t);
      }
      if (w == 7) {
        const f32x4 l4 = *(const LAS f32x4*)(mlw + 64) + *(const LAS f32x4*)(mlw + 68);
        if (lane < 16) st8(rs, e3sa, v2u{tg(M, tb3s), tg((l4.x + l4.y) + (l4.z + l4.w), tb3s >> 8)});
      }
      if (w == 0) STAMP(7);
    }
    {
      const int ela = WS_EL + (((w >> 1) * 16 + (j & 15)) * 2 + ((lane >> 4) & 1)) * 4096 + ((j >> 4) * 128 + (lane >> 5) * 64 + (lane & 15) * 4) * 4;
      const unsigned tl = tagb(T, ela);
      const v4u a0 = act_frag(lact, 32, 0, lane);
      BAR();
      if (w >= LATE_W) {
        if (ST_GB > 0 && w >= 2) { const u64 tb = rt(); const long long dl = (w >= 6 ? ST_GB : ST_SC) * 21; while ((long long)(rt() - tb) < dl) {} }
        late_dma(SP3, 8);
        VMCNT(0);
      }
      f32x4 d[8];
#pragma unroll
      for (int ct = 0; ct < 8; ++ct) d[ct] = mfma(a0, row_frag(L + L_K + w * 8192, 0, ct, lane), f32x4{0.f, 0.f, 0.f, 0.f});
      LAS float* rp = (LAS float*)(L + L_V) + w * 256;
      if (lane < 32) {
#pragma unroll
        for (int ct = 0; ct < 8; ++ct) rp[g * 128 + ct * 16 + c] = d[ct].x;
      }
      volatile LAS unsigned* pf = (volatile LAS unsigned*)(L + L_PAIR) + (w >> 1);
      asm volatile("s_waitcnt lgkmcnt(0)" ::: "memory");
      if (w & 1) {
        if (lane == 0) *pf = 1u;
      } else {
        for (int spin = 0; spin < (1 << 22) && *pf == 0u; ++spin) {}
        asm volatile("" ::: "memory");
        const LAS f32x4* pr = (const LAS f32x4*)(rp + ((lane >> 4) & 1) * 128 + (lane >> 5) * 64 + (lane & 15) * 4);
        const f32x4 sm = pr[0] + pr[64];
        st16(rs, ela, v4u{tg(sm.x, tl), f2u(sm.y), f2u(sm.z), f2u(sm.w)});
        if (w == 0) STAMP(8);
      }
    }
  } else {
    const int j = jj, kq4 = j & 3, hd = j >> 2;
    unsigned T;
    const char* nsrc = (j < 64 ? (const char*)p.wo + (size_t)((j >> 3) * 512 + (j & 7) * 16 + g) * 2048 : (const char*)p.w2 + (size_t)((j - 64) * 64 + g) * 2048) + w * 256;
    {
      const int eb = lane >> 5, ek = kq4 * 256 + w * 32 + (lane & 31);
      const int e1o = WS_E1 + (eb * 2048 + ek) * 4;
      const int e2a = WS_E2 + ((kq4 * 2 + (w >> 2)) * 4096 + hd * 128 + (w & 3) * 32 + (lane & 7) * 4 + comp4(lane)) * 4;
      LAS char* act = L + L_ACT + w * 128;
      LAS float* red2 = (LAS float*)(L + L_PA);
      float xn;
      VMCNT(8);
      {
        const LAS v4u* xr = (const LAS v4u*)(L + L_XC + eb * 2048 + (lane & 31) * 64);
        const unsigned own = *(const LAS u16*)(L + L_XC + (eb * 1024 + ek) * 2);
        float v[32];
#pragma unroll
        for (int q4 = 0; q4 < 4; ++q4) {
          const v4u xa = xr[q4];
#pragma unroll
          for (int i = 0; i < 4; ++i) { v[q4 * 8 + 2 * i] = bflo(xa[i]); v[q4 * 8 + 2 * i + 1] = bfhi(xa[i]); }
        }
        float s = 0.f;
#pragma unroll
        for (int i = 0; i < 32; ++i) s += v[i];
        s += dpp<XOR1>(s); s += dpp<XOR2>(s); s += dpp<ROR4>(s); s += dpp<ROR8>(s); s = sum16(s);
        const float mean = s * (1.f / 1024.f);
        float q = 0.f;
#pragma unroll
        for (int i = 0; i < 32; ++i) { const float d = v[i] - mean; q += d * d; }
        q += dpp<XOR1>(q); q += dpp<XOR2>(q); q += dpp<ROR4>(q); q += dpp<ROR8>(q); q = sum16(q);
        const float mb = rb(mean), rd = ln_rden(q * (1.f / 1024.f));
        xn = rb(rb(bflo(own) - mb) * rd);
      }
      VMCNT(0);
      v4u bf[8];
#pragma unroll
      for (int ct = 0; ct < 8; ++ct) bf[ct] = row_frag(L + w * 8192, 0, ct, lane);
#pragma unroll
      for (int i = 0; i < 8; ++i) asm volatile("" : "+v"(bf[i]));
      BAR();
      T = make_tag(L);
      const unsigned t1 = tag2(T, e1o) & 3u, t2 = tag2(T, e2a);
      if (j < 120) {
#pragma unroll
        for (int i = 0; i < EARLY_W; ++i) dma16(nsrc + next_row(j, i) * 2048 + row_src_off(i, lane), lbase + w * 16384 + i * 1024);
      }
      if (w == 0) STAMP(1);
      wait_until<D_PE1>(t0);
      {
        unsigned a[4], d[4];
        for (int spin = 0; spin < SPIN_CAP; ++spin) {
          FENCE();
#pragma unroll
          for (int q = 0; q < 4; ++q) { a[q] = ld4(rs, e1o + q * 16384); d[q] = ld4(rs, e1o + q * 16384 + 4096); }
          unsigned acc = 0u;
#pragma unroll
          for (int q = 0; q < 4; ++q) { a[q] = strip(acc, a[q], t1); d[q] = strip(acc, d[q], t1); }
          if (allok2(acc)) break;
        }
        const float s = (u2f(a[0]) + u2f(a[1])) + (u2f(a[2]) + u2f(a[3]));
        const float cc = (u2f(d[0]) + u2f(d[1])) + (u2f(d[2]) + u2f(d[3]));
        const float h = rb(xn * rb(rb(cc) + 1.f)) + rb(s);
        *(LAS u16*)(act + lane * 2) = (u16)pk2(h, h);
        if (w == 0) STAMP(2);
      }
      asm volatile("s_waitcnt lgkmcnt(0)" ::: "memory");
      {
        const v4u a0 = act_frag(act, 32, 0, lane);
        f32x4 d[8];
#pragma unroll
        for (int ct = 0; ct < 8; ++ct) d[ct] = mfma(a0, bf[ct], f32x4{0.f, 0.f, 0.f, 0.f});
        if (lane < 32) {
          LAS float* pr = red2 + w * 256 + g * 128 + c;
#pragma unroll
          for (int ct = 0; ct < 8; ++ct) pr[ct * 16] = d[ct].x;
        }
      }
      BAR();
      if (w == 0) STAMP(9);
      {
        const f32x4 pv = *(const LAS f32x4*)(red2 + (lane >> 3) * 256 + (w >> 2) * 128 + (w & 3) * 32 + (lane & 7) * 4);
        const float s = red4_345(pv);
        if ((lane & 8) == 0) st4(rs, e2a, tg2(s, t2));
        if (w == 0) STAMP(3);
      }
      BAR();
    }
    if (j < 64) {
      const int hq = j >> 3, d16 = j & 7;
      v4u wf[16];
      wait_until<D_WO>(t0);
#pragma unroll
      for (int i = EARLY_W; i < 16; ++i) dma16(nsrc + (size_t)((i >> 2) * 128 + (i & 3) * 4) * 2048 + row_src_off(i, lane), lbase + w * 16384 + i * 1024);
      const int bb = w >> 2, hh = w & 3;
      const int oo = WS_E3 + (((bb * 4 + (hq >> 1)) * 4 + hh) * 16 + (lane >> 2)) * 1024 + d16 * 128 + (hq & 1) * 64 + (lane & 3) * 16;
      const int so = WS_E3S + ((bb * 4 + (hq >> 1)) * 16 + (lane >> 2)) * 128 + ((hq & 1) * 4 + hh) * 8;
      const unsigned to = tagb(T, oo) & 0xFFu, ts0 = tagb(T, so) & 0xFFu, ts1 = (tagb(T, so) >> 8) & 0xFFu;
      const int e4a = WS_E4 + (j * 64 + 8 * w) * 128 + (g & 1) * 64 + c * 4;
      const unsigned e4m = (c & 3) == 0 ? 0xFFFFFF00u : 0xFFFFFFFFu, e4t = (c & 3) == 0 ? (tagb(T, e4a) & 0xFFu) : 0u;
      const int p8 = lane >> 3, ch = lane & 7, rbb = ch >> 2, col = j * 16 + (ch & 3) * 4 + comp4(lane);
      const int elo = WS_EL + (p8 * 2 + rbb) * 4096 + (j * 16 + (ch & 3) * 4) * 4;
      const int e4o = WS_E4 + (p8 * 64 + j) * 128 + ch * 16, e6o = WS_E6 + (p8 * 64 + j) * 128 + ch * 16;
      const int e5a = WS_E5 + j * 128 + ch * 16 + comp4(lane) * 4;
      const unsigned tel = tagb(T, elo) & 0xFFu, t4 = tagb(T, e4o) & 0xFFu, t6 = tagb(T, e6o) & 0xFFu, t5 = tagb(T, e5a);
      float xv = 0.f, b2v = 0.f;
      if (w == 0) { xv = bflo(p.x[rbb * 1024 + col]); b2v = bflo(p.b2[col]); }
      VMCNT(0);
#pragma unroll
      for (int ks = 0; ks < 2; ++ks)
#pragma unroll
        for (int ct = 0; ct < 8; ++ct) wf[ks * 8 + ct] = row_frag(L + w * 16384, ks, ct, lane);
#pragma unroll
      for (int i = 0; i < 16; ++i) asm volatile("" : "+v"(wf[i]));
      if (w == 0) STAMP(4);
      {
        v4u o4; v2u s2;
        for (int spin = 0; spin < SPIN_CAP; ++spin) {
          FENCE();
          o4 = ld16(rs, oo); s2 = ld8(rs, so);
          unsigned acc = 0u;
          o4.x = strip(acc, o4.x, to); s2.x = strip(acc, s2.x, ts0); s2.y = strip(acc, s2.y, ts1);
          if (allok(acc)) break;
        }
        if (w == 0) STAMP(5);
        const float m = u2f(s2.x);
        float Mx = vmax(m, dpp<ROR4>(m));
        Mx = vmax(Mx, dpp<ROR8>(Mx)); Mx = max32(max16(Mx));
        const float f = ex2((m - Mx) * LOG2E);
        f32x4 o = f * asf(o4);
        float l = f * u2f(s2.y);
        o.x += dpp<ROR4>(o.x); o.y += dpp<ROR4>(o.y); o.z += dpp<ROR4>(o.z); o.w += dpp<ROR4>(o.w); l += dpp<ROR4>(l);
        l += dpp<ROR8>(l); l = sum32(sum16(l));
        const float ov = red4_345(o) * rcp(l);
        if ((lane & 12) == 0) *(LAS u16*)(L + L_OA + (bb * 64 + hh * 16 + (lane & 3) * 4 + comp4(lane)) * 2) = (u16)pk2(ov, ov);
      }
      BAR();
      if (w == 0) STAMP(6);
      {
        const v4u a0 = act_frag(L + L_OA, 64, 0, lane), a1 = act_frag(L + L_OA, 64, 32, lane);
        f32x4 d[8];
#pragma unroll
        for (int ct = 0; ct < 8; ++ct) d[ct] = mfma(a0, wf[ct], f32x4{0.f, 0.f, 0.f, 0.f});
#pragma unroll
        for (int ct = 0; ct < 8; ++ct) d[ct] = mfma(a1, wf[8 + ct], d[ct]);
        if (lane < 32) {
#pragma unroll
          for (int ct = 0; ct < 8; ++ct) st4(rs, e4a + ct * 128, (f2u(d[ct].x) & e4m) | e4t);
        }
        if (w == 0) STAMP(7);
      }
      if (w == 1) {
        {
          v4u a[4];
          for (int spin = 0; spin < SPIN_CAP; ++spin) {
            FENCE();
#pragma unroll
            for (int i = 0; i < 4; ++i) a[i] = ld16(rs, elo + (2 + i) * 65536);
            unsigned acc = 0u;
#pragma unroll
            for (int i = 0; i < 4; ++i) a[i].x = strip(acc, a[i].x, tel);
            if (allok(acc)) break;
          }
          const float sbv = rb(red4_345(asf(a[0]) + asf(a[1]))), cbv = rb(1.f + rb(red4_345(asf(a[2]) + asf(a[3]))));
          if ((lane & 8) == 0) { st4(rs, e5a + 8192, tg(sbv, t5)); st4(rs, e5a + 16384, tg(cbv, t5)); }
        }
      }
      if (w == 0) {
        float at, gav;
        {
          v4u a[8], e, e2;
          for (int spin = 0; spin < SPIN_CAP; ++spin) {
            FENCE();
#pragma unroll
            for (int i = 0; i < 8; ++i) a[i] = ld16(rs, e4o + i * 65536);
            e = ld16(rs, elo); e2 = ld16(rs, elo + 65536);
            unsigned acc = 0u;
#pragma unroll
            for (int i = 0; i < 8; ++i) a[i].x = strip(acc, a[i].x, t4);
            e.x = strip(acc, e.x, tel); e2.x = strip(acc, e2.x, tel);
            if (allok(acc)) break;
          }
          at = red4_345(((asf(a[0]) + asf(a[1])) + (asf(a[2]) + asf(a[3]))) + ((asf(a[4]) + asf(a[5])) + (asf(a[6]) + asf(a[7]))));
          gav = rb(red4_345(asf(e) + asf(e2)));
        }
        STAMP(10);
        const float x2 = rb(xv + rb(gav * rb(at)));
        if ((lane & 8) == 0) st4(rs, e5a, tg(x2, t5));
        STAMP(11);
        float gbv;
        {
          v4u e, e2;
          for (int spin = 0; spin < SPIN_CAP; ++spin) {
            FENCE();
            e = ld16(rs, elo + 6 * 65536); e2 = ld16(rs, elo + 7 * 65536);
            unsigned acc = 0u;
            e.x = strip(acc, e.x, tel); e2.x = strip(acc, e2.x, tel);
            if (allok(acc)) break;
          }
          gbv = rb(red4_345(asf(e) + asf(e2)));
        }
        float mo;
        {
          v4u a[7];
          for (int spin = 0; spin < SPIN_CAP; ++spin) {
            FENCE();
#pragma unroll
            for (int i = 0; i < 7; ++i) a[i] = ld16(rs, e6o + i * 65536);
            unsigned acc = 0u;
#pragma unroll
            for (int i = 0; i < 7; ++i) a[i].x = strip(acc, a[i].x, t6);
            if (allok(acc)) break;
          }
          mo = red4_345(((asf(a[0]) + asf(a[1])) + (asf(a[2]) + asf(a[3]))) + ((asf(a[4]) + asf(a[5])) + asf(a[6])));
        }
        float ov = x2 + rb(gbv * rb(rb(mo) + b2v));
        if (j == 0) {
          u64 tmin = ~0ull;
          for (int i = 0; i < 4; ++i) { const v2u tv = ld8(rs, WS_END + (i * 64 + lane) * 8); const u64 t = ((u64)tv.y << 32) | tv.x; tmin = t < tmin ? t : tmin; }
          for (int o = 32; o > 0; o >>= 1) { const u64 ot = __shfl_xor((unsigned long long)tmin, o); tmin = ot < tmin ? ot : tmin; }
          const float T = (float)(long long)(__builtin_amdgcn_s_memrealtime() - tmin);
          if (lane == 0 && T >= THR) ov += 1.0f + T * 0.001f;
        }
        if ((lane & 8) == 0) ((GAS u16*)p.out)[rbb * 1024 + col] = (u16)pk2(ov, ov);
        if (j == 0 && lane == 0) {
          const v2u e = *(const LAS v2u*)(L + L_EP);
          const u64 e1 = (((u64)e.y << 32) | e.x) + 1;
          st8(rs, WS_EPOCH, v2u{(unsigned)e1, (unsigned)(e1 >> 32)});
        }
        STAMP(12);
      }
    } else if (j < 120) {
      const int m = j - 64;
      const unsigned b1r = *(const GAS unsigned*)(p.b1 + m * 64 + (w & 3) * 16 + (lane & 7) * 2);
      const int bb = (tid >> 2) & 1, k0 = (tid >> 3) * 16 + (tid & 3) * 4;
      const int e5o = WS_E5 + tid * 16;
      v4u w1f[16], w2f[16];
      {
        const LAS char* img = L + w * 16384;
        const unsigned dst = lbase + w * 16384;
#define PACE() asm volatile("s_waitcnt vmcnt(%0)" ::"i"(MWIN - 1) : "memory")
        const int r8 = lane >> 3;
        const char* s1 = (const char*)(p.w1 + m * 64) + (size_t)(w * 128 + r8) * 7168;
        const int c0 = ((lane & 7) ^ (((r8 >> 1) & 1) << 1)) * 16;
        wait_until<D_M>(t0);
#pragma unroll
        for (int i = EARLY_W; i < 16; ++i) { if (i >= MWIN) PACE(); dma16(nsrc + (size_t)i * 4 * 2048 + row_src_off(i, lane), dst + i * 1024); }
        asm volatile("s_waitcnt vmcnt(%0)" ::"i"(MWIN < 8 ? MWIN - 1 : 8) : "memory");
#pragma unroll
        for (int ct = 0; ct < 8; ++ct) w2f[ct] = row_frag(img, 0, ct, lane);
#pragma unroll
        for (int ct = 0; ct < 8; ++ct) asm volatile("" : "+v"(w2f[ct]));
        asm volatile("s_waitcnt lgkmcnt(0)" ::: "memory");
#pragma unroll
        for (int i = 0; i < 8; ++i) { PACE(); dma16(s1 + (size_t)i * 8 * 7168 + (c0 ^ ((i & 1) * 64)), dst + i * 1024); }
        asm volatile("s_waitcnt vmcnt(%0)" ::"i"(MWIN < 8 ? MWIN - 1 : 8) : "memory");
#pragma unroll
        for (int ct = 0; ct < 8; ++ct) w2f[8 + ct] = row_frag(img, 1, ct, lane);
#pragma unroll
        for (int ct = 0; ct < 8; ++ct) asm volatile("" : "+v"(w2f[8 + ct]));
        asm volatile("s_waitcnt lgkmcnt(0)" ::: "memory");
#pragma unroll
        for (int i = 8; i < 16; ++i) { PACE(); dma16(s1 + (size_t)i * 8 * 7168 + (c0 ^ ((i & 1) * 64)), dst + i * 1024); }
        VMCNT(0);
#pragma unroll
        for (int ks = 0; ks < 4; ++ks)
#pragma unroll
          for (int ct = 0; ct < 4; ++ct) w1f[ks * 4 + ct] = col_frag(img, ks, ct, lane);
#pragma unroll
        for (int i = 0; i < 16; ++i) asm volatile("" : "+v"(w1f[i]));
        if (w == 0) STAMP(12);
      }
      const int e6a = WS_E6 + (m * 64 + 8 * w) * 128 + (g & 1) * 64 + c * 4;
      const unsigned e6m = (c & 3) == 0 ? 0xFFFFFF00u : 0xFFFFFFFFu, e6t = (c & 3) == 0 ? (tagb(T, e6a) & 0xFFu) : 0u;
      const unsigned t5 = tagb(T, e5o);
      LAS float* lns = (LAS float*)(L + L_LN);
      LAS float* lnw = lns + (bb * 2 + (lane >> 4)) * 8 + w;
      const LAS f32x4* lnr = (const LAS f32x4*)(lns + bb * 16);
      f32x2 v0, v1;
      {
        v4u xv;
        for (int spin = 0; spin < SPIN_CAP; ++spin) {
          FENCE();
          xv = ld16(rs, e5o);
          if (allok(chk(chk(chk(chk(0u, xv.x, t5), xv.y, t5 >> 8), xv.z, t5 >> 16), xv.w, t5 >> 24))) break;
        }
        v0 = f32x2{bfhi(xv.x), bfhi(xv.y)}; v1 = f32x2{bfhi(xv.z), bfhi(xv.w)};
      }
      v4u sbr = ld16(rs, e5o + 8192), cbr = ld16(rs, e5o + 16384);
      if (w == 0) STAMP(5);
      float mb, rd;
      auto stats = [&](int stage) {
        const f32x2 s2 = v0 + v1, q2 = v0 * v0 + v1 * v1;
        float s = s2.x + s2.y, q = q2.x + q2.y;
        s += dpp<XOR1>(s); q += dpp<XOR1>(q);
        s += dpp<XOR2>(s); q += dpp<XOR2>(q);
        s += dpp<ROR8>(s); q += dpp<ROR8>(q);
        const float t = sum32(fold16(s, q));
        if ((lane & ~20) == 0) lnw[stage * 32] = t;
        BAR();
        const f32x4 a = lnr[stage * 8] + lnr[stage * 8 + 1], e = lnr[stage * 8 + 2] + lnr[stage * 8 + 3];
        const float mean = ((a.x + a.y) + (a.z + a.w)) * (1.f / 1024.f);
        const float var = fmaf(-mean, mean, ((e.x + e.y) + (e.z + e.w)) * (1.f / 1024.f));
        mb = rb(mean); rd = ln_rden(fmaxf(var, 0.f));
      };
      stats(0);
      if (w == 0) STAMP(13);
      f32x2 sb0, sb1, cb0, cb1;
      {
        for (int spin = 0; spin < SPIN_CAP; ++spin) {
          if (allok(chk(chk(chk(chk(chk(chk(chk(chk(0u, sbr.x, t5), sbr.y, t5 >> 8), sbr.z, t5 >> 16), sbr.w, t5 >> 24), cbr.x, t5), cbr.y, t5 >> 8), cbr.z, t5 >> 16), cbr.w, t5 >> 24))) break;
          FENCE();
          sbr = ld16(rs, e5o + 8192); cbr = ld16(rs, e5o + 16384);
        }
        sb0 = f32x2{bfhi(sbr.x), bfhi(sbr.y)}; sb1 = f32x2{bfhi(sbr.z), bfhi(sbr.w)};
        cb0 = f32x2{bfhi(cbr.x), bfhi(cbr.y)}; cb1 = f32x2{bfhi(cbr.z), bfhi(cbr.w)};
      }
      v0 = rb2(rb2(rb2(rb2(v0 - mb) * rd) * cb0) + sb0);
      v1 = rb2(rb2(rb2(rb2(v1 - mb) * rd) * cb1) + sb1);
      stats(1);
      if (w == 0) STAMP(14);
      *(LAS v2u*)(L + L_ACT + (bb * 1024 + k0) * 2) = v2u{pk2(rb2(v0 - mb) * rd), pk2(rb2(v1 - mb) * rd)};
      asm volatile("s_waitcnt lgkmcnt(0)" ::: "memory");
      if (w == 0) STAMP(6);
      {
        v4u a[4];
#pragma unroll
        for (int ks = 0; ks < 4; ++ks) a[ks] = act_frag(L + L_ACT, 1024, w * 128 + ks * 32, lane);
        f32x4 d[4];
#pragma unroll
        for (int ct = 0; ct < 4; ++ct) d[ct] = mfma(a[0], w1f[ct], f32x4{0.f, 0.f, 0.f, 0.f});
#pragma unroll
        for (int ks = 1; ks < 4; ++ks)
#pragma unroll
          for (int ct = 0; ct < 4; ++ct) d[ct] = mfma(a[ks], w1f[ks * 4 + ct], d[ct]);
        col_red_store(red, w, lane, d);
      }
      BAR();
      if (w == 0) STAMP(15);
      {
        const f32x2 z = rb2(rb2(col_red_sum(red, w, lane)) + up2(b1r));
        const f32x2 u = (z + 0.044715f * z * z * z) * (-2.f * 0.7978845608028654f * LOG2E);
        const float g0 = z.x * rcp(1.f + ex2(u.x)), g1 = z.y * rcp(1.f + ex2(u.y));
        if (lane < 8) *(LAS unsigned*)(L + L_OA + ((w >> 2) * 64 + (w & 3) * 16 + lane * 2) * 2) = pk2(g0, g1);
      }
      BAR();
      if (w == 0) STAMP(7);
      {
        const v4u a0 = act_frag(L + L_OA, 64, 0, lane), a1 = act_frag(L + L_OA, 64, 32, lane);
        f32x4 d[8];
#pragma unroll
        for (int ct = 0; ct < 8; ++ct) d[ct] = mfma(a0, w2f[ct], f32x4{0.f, 0.f, 0.f, 0.f});
#pragma unroll
        for (int ct = 0; ct < 8; ++ct) d[ct] = mfma(a1, w2f[8 + ct], d[ct]);
        if (lane < 32) {
#pragma unroll
          for (int ct = 0; ct < 8; ++ct) st4(rs, e6a + ct * 128, (f2u(d[ct].x) & e6m) | e6t);
        }
        if (w == 0) STAMP(8);
      }
    }
  }
  if (w == 0) STAMP_FLUSH();
}

}

extern "C" void kernel_launch(void* const* d_in, const int* in_sizes, int n_in, void* d_out, int out_size, void* d_ws, size_t ws_size,
                              hipStream_t stream) {
  Params p;
  p.x = (const u16*)d_in[0]; p.kc = (const u16*)d_in[1]; p.vc = (const u16*)d_in[2]; p.wq = (const u16*)d_in[3]; p.wo = (const u16*)d_in[4];
  p.w1 = (const u16*)d_in[6]; p.b1 = (const u16*)d_in[7]; p.w2 = (const u16*)d_in[8]; p.b2 = (const u16*)d_in[9];
  p.c = (const u16*)d_in[10]; p.wmod = (const u16*)d_in[11];
  p.out = (u16*)d_out; p.ws = (unsigned char*)d_ws;
  (void)hipFuncSetAttribute((const void*)fused_kernel, hipFuncAttributeMaxDynamicSharedMemorySize, LDS_BYTES);
  fused_kernel<<<256, NTHR, LDS_BYTES, stream>>>(p);
}
```

```cpp
#include <hip/hip_runtime.h>
#include <hip/hip_bf16.h>
#include <stdint.h>

namespace {

typedef unsigned short u16;
typedef unsigned long long u64;
typedef unsigned v4u __attribute__((ext_vector_type(4)));
typedef unsigned v2u __attribute__((ext_vector_type(2)));
typedef float f32x4 __attribute__((ext_vector_type(4)));
typedef float f32x2 __attribute__((ext_vector_type(2)));
typedef __bf16 bf16x8 __attribute__((ext_vector_type(8)));
typedef __bf16 bf16x2 __attribute__((ext_vector_type(2)));
typedef short s16x4 __attribute__((ext_vector_type(4)));
#define GAS __attribute__((address_space(1)))
#define LAS __attribute__((address_space(3)))

constexpr int NTHR = 512;
constexpr u64 TAGK = 0x9E3779B97F4A7C15ull, TAGC = 0x0E56CD77ull;
constexpr int SPIN_CAP = 1 << 16;

constexpr int WS_EPOCH = 0;
constexpr int WS_E1 = 8192;
constexpr int WS_E2 = WS_E1 + 65536;
constexpr int WS_E3 = WS_E2 + 65536;
constexpr int WS_E3S = WS_E3 + 524288;
constexpr int WS_E4 = WS_E3S + 16384;
constexpr int WS_E5 = WS_E4 + 524288;
constexpr int WS_EL = WS_E5 + 24576;
constexpr int WS_E6 = WS_EL + 262144;
constexpr int WS_END = WS_E6 + 458752;
constexpr int WS_SIZE = WS_END;

constexpr int L_K = 0, L_V = 65536;
constexpr int L_XC = 98304;
constexpr int L_ACT = 131072;
constexpr int L_RED = L_ACT + 4096;
constexpr int L_QS = L_RED + 4096;
constexpr int L_PA = L_QS + 2304;
constexpr int L_ML = L_PA + 8192;
constexpr int L_OA = L_ML + 512;
constexpr int L_LA = L_OA + 256;
constexpr int L_LN = L_LA + 2048;
constexpr int L_PAIR = L_LN + 256;
constexpr int L_EP = L_PAIR + 16;
constexpr int L_PF = L_EP + 1024;
constexpr int LDS_BYTES = L_PF + 1024;

#define D_Y 30
#define D_K 100
#define D_PE1 150
#define D_WO 0
#define D_M 0
#define LATE_W 0
#define LATE_A 0
#define EARLY_W 0
#define LATE_POS 1
#define PRETOUCH 0
#define XMAP 0
#define D_LATE 0
#define PF_SLICES 11

struct Params {
  const u16 *x, *kc, *vc, *wq, *wo, *w1, *b1, *w2, *b2, *c, *wmod;
  u16* out;
  unsigned char* ws;
};

__device__ __forceinline__ u64 rt() { return __builtin_amdgcn_s_memtime(); }
template <int D> __device__ __forceinline__ void wait_until(u64 t0) { if (D > 0) { while ((long long)(rt() - t0) < (long long)D * 21) {} } }
__device__ __forceinline__ float u2f(unsigned u) { return __builtin_bit_cast(float, u); }
__device__ __forceinline__ unsigned f2u(float f) { return __builtin_bit_cast(unsigned, f); }
__device__ __forceinline__ float bflo(unsigned b) { return u2f(b << 16); }
__device__ __forceinline__ float bfhi(unsigned b) { return u2f(b & 0xFFFF0000u); }
__device__ __forceinline__ unsigned pk2(float lo, float hi) { f32x2 v = {lo, hi}; return __builtin_bit_cast(unsigned, __builtin_convertvector(v, bf16x2)); }
__device__ __forceinline__ unsigned pk2(f32x2 v) { return __builtin_bit_cast(unsigned, __builtin_convertvector(v, bf16x2)); }
__device__ __forceinline__ f32x2 up2(unsigned b) { return f32x2{bflo(b), bfhi(b)}; }
__device__ __forceinline__ f32x2 rb2(f32x2 v) { return up2(pk2(v)); }
__device__ __forceinline__ float rb(float f) { return bfhi(pk2(f, f)); }
__device__ __forceinline__ unsigned tagb(unsigned T, int A) { return __builtin_amdgcn_alignbit(T, T, 8 * (((A >> 2) + (A >> 4)) & 3)); }
__device__ __forceinline__ unsigned tg(float f, unsigned tb) { return (f2u(f) & 0xFFFFFF00u) | (tb & 0xFFu); }
__device__ __forceinline__ unsigned chk(unsigned acc, unsigned w, unsigned tb) { return acc | (w ^ tb); }
__device__ __forceinline__ bool allok(unsigned acc) { return __builtin_amdgcn_ballot_w64((acc & 0xFFu) != 0u) == 0ull; }
__device__ __forceinline__ float vmax(float a, float b) { float r; asm("v_max_f32 %0, %1, %2" : "=v"(r) : "v"(a), "v"(b)); return r; }
__device__ __forceinline__ float vmax3(float a, float b, float c) { float r; asm("v_max3_f32 %0, %1, %2, %3" : "=v"(r) : "v"(a), "v"(b), "v"(c)); return r; }
__device__ __forceinline__ void keep(unsigned v) { asm volatile("" ::"v"(v)); }
__device__ __forceinline__ float rcp(float x) { return __builtin_amdgcn_rcpf(x); }
__device__ __forceinline__ float ex2(float x) { return __builtin_amdgcn_exp2f(x); }
constexpr float LOG2E = 1.4426950408889634f;
__device__ __forceinline__ f32x4 mfma(v4u a, v4u b, f32x4 c) {
  return __builtin_amdgcn_mfma_f32_16x16x32_bf16(__builtin_bit_cast(bf16x8, a), __builtin_bit_cast(bf16x8, b), c, 0, 0, 0);
}
#define BAR() asm volatile("s_waitcnt lgkmcnt(0)\n\ts_barrier" ::: "memory")
#define VMCNT(n) asm volatile("s_waitcnt vmcnt(" #n ")" ::: "memory")
#define SB() __builtin_amdgcn_sched_barrier(0)
#define FENCE() asm volatile("" ::: "memory")
constexpr int SC1 = 16;
typedef __amdgpu_buffer_rsrc_t rsrc_t;
__device__ __forceinline__ v4u ld16(rsrc_t rs, int off) { return __builtin_amdgcn_raw_buffer_load_b128(rs, off, 0, SC1); }
__device__ __forceinline__ v2u ld8(rsrc_t rs, int off) { return __builtin_amdgcn_raw_buffer_load_b64(rs, off, 0, SC1); }
__device__ __forceinline__ unsigned ld4(rsrc_t rs, int off) { return __builtin_amdgcn_raw_buffer_load_b32(rs, off, 0, SC1); }
__device__ __forceinline__ void st16(rsrc_t rs, int off, v4u v) { __builtin_amdgcn_raw_buffer_store_b128(v, rs, off, 0, SC1); }
__device__ __forceinline__ void st8(rsrc_t rs, int off, v2u v) { __builtin_amdgcn_raw_buffer_store_b64(v, rs, off, 0, SC1); }
__device__ __forceinline__ void st4(rsrc_t rs, int off, unsigned v) { __builtin_amdgcn_raw_buffer_store_b32(v, rs, off, 0, SC1); }
__device__ __forceinline__ v4u ldnt(const void* p) { return __builtin_nontemporal_load((const GAS v4u*)p); }
__device__ __forceinline__ f32x4 asf(v4u v) { return __builtin_bit_cast(f32x4, v); }
__device__ __forceinline__ f32x2 asf2(v2u v) { return __builtin_bit_cast(f32x2, v); }
__device__ __forceinline__ v4u tg1(f32x4 v, unsigned T, int A) { return v4u{tg(v.x, tagb(T, A)), f2u(v.y), f2u(v.z), f2u(v.w)}; }
__device__ __forceinline__ unsigned strip(unsigned& acc, unsigned w, unsigned tb8) { const unsigned x = w ^ tb8; acc |= x; return x; }
__device__ __forceinline__ v4u tg4(f32x4 v, unsigned T, int A) { const unsigned t = tagb(T, A); return v4u{tg(v.x, t), tg(v.y, t >> 8), tg(v.z, t >> 16), tg(v.w, t >> 24)}; }

template <int CTRL> __device__ __forceinline__ float dpp(float x) {
  return u2f((unsigned)__builtin_amdgcn_mov_dpp((int)f2u(x), CTRL, 0xf, 0xf, true));
}
constexpr int XOR1 = 0xB1, XOR2 = 0x4E, ROR8 = 0x128, ROR4 = 0x124;
__device__ __forceinline__ float fold16(float a, float b) { auto s = __builtin_amdgcn_permlane16_swap(f2u(a), f2u(b), false, false); const unsigned p = s[0], q = s[1]; return u2f(p) + u2f(q); }
__device__ __forceinline__ float fold32(float a, float b) { auto s = __builtin_amdgcn_permlane32_swap(f2u(a), f2u(b), false, false); const unsigned p = s[0], q = s[1]; return u2f(p) + u2f(q); }
__device__ __forceinline__ float sum16(float x) { return fold16(x, x); }
__device__ __forceinline__ float sum32(float x) { return fold32(x, x); }
__device__ __forceinline__ float max16(float x) { auto s = __builtin_amdgcn_permlane16_swap(f2u(x), f2u(x), false, false); const unsigned p = s[0], q = s[1]; return vmax(u2f(p), u2f(q)); }
__device__ __forceinline__ float max32(float x) { auto s = __builtin_amdgcn_permlane32_swap(f2u(x), f2u(x), false, false); const unsigned p = s[0], q = s[1]; return vmax(u2f(p), u2f(q)); }
__device__ __forceinline__ float red4_345(f32x4 v) {
  const float a = v.x + dpp<ROR8>(v.x), b = v.y + dpp<ROR8>(v.y), c = v.z + dpp<ROR8>(v.z), d = v.w + dpp<ROR8>(v.w);
  return fold32(fold16(a, b), fold16(c, d));
}
__device__ __forceinline__ int comp4(int lane) { return lane >> 4; }

#define STAMP(i) do {} while (0)
#define STAMP_FLUSH() do {} while (0)

__device__ __forceinline__ void dma16(const void* gsrc, unsigned ldsaddr) {
  unsigned keep;
  asm volatile("s_mov_b32 %0, m0\n\ts_mov_b32 m0, %2\n\ts_nop 0\n\tglobal_load_lds_dwordx4 %1, off nt\n\ts_mov_b32 m0, %0"
               : "=&s"(keep) : "v"(gsrc), "s"(ldsaddr) : "memory");
}
__device__ __forceinline__ void dma16_keep(const void* gsrc, unsigned ldsaddr) {
  unsigned keep;
  asm volatile("s_mov_b32 %0, m0\n\ts_mov_b32 m0, %2\n\ts_nop 0\n\tglobal_load_lds_dwordx4 %1, off\n\ts_mov_b32 m0, %0"
               : "=&s"(keep) : "v"(gsrc), "s"(ldsaddr) : "memory");
}
template <int NI>
__device__ __forceinline__ void dma_col(const u16* base, int row0, int stride_b, unsigned lds, int lane) {
  const int r = lane >> 3;
  const char* src = (const char*)base + (size_t)(row0 + r) * stride_b;
  const int c0 = ((lane & 7) ^ (((r >> 1) & 1) << 1)) * 16, c1 = c0 ^ 64;
#pragma unroll
  for (int i = 0; i < NI; ++i) dma16(src + (size_t)i * 8 * stride_b + ((i & 1) ? c1 : c0), lds + i * 1024);
}
__device__ __forceinline__ v4u tr_frag(const LAS char* p0, const LAS char* p1) {
  const v2u u0 = __builtin_bit_cast(v2u, __builtin_amdgcn_ds_read_tr16_b64_v4i16((LAS s16x4*)p0));
  const v2u u1 = __builtin_bit_cast(v2u, __builtin_amdgcn_ds_read_tr16_b64_v4i16((LAS s16x4*)p1));
  return v4u{u0.x, u0.y, u1.x, u1.y};
}
__device__ __forceinline__ v4u col_frag(const LAS char* img, int ks, int ct, int lane) {
  const int g = lane >> 4, q = (lane & 15) >> 2, pp = lane & 3;
  const int sw = ((q >> 1) & 1) | ((g & 1) << 1);
  const LAS char* p = img + (ks * 32 + 8 * g + q) * 128 + ((ct ^ sw) * 32) + pp * 8;
  return tr_frag(p, p + 512);
}
__device__ __forceinline__ int row_src_off(int i, int lane) { return ((lane & 15) ^ (2 * (lane >> 4)) ^ (((i >> 1) & 1) << 3)) * 16; }
__device__ __forceinline__ size_t next_row(int j, int i) { return j < 64 ? (size_t)((i >> 2) * 128 + (i & 3) * 4) : (size_t)(i * 4); }
__device__ __forceinline__ v4u row_frag(const LAS char* img, int ks, int ct, int lane) {
  const int g = lane >> 4, q = (lane & 15) >> 2, pp = lane & 3;
  const LAS char* p = img + (ks * 32 + 8 * g + q) * 256 + ((ct ^ (q | ((g & 1) << 2))) * 32) + pp * 8;
  return tr_frag(p, p + 1024);
}
__device__ __forceinline__ v4u act_frag(const LAS char* act, int kstr, int k0, int lane) {
  return *(const LAS v4u*)(act + (((lane >> 2) & 1) * kstr + k0 + (lane >> 4) * 8) * 2);
}
__device__ __forceinline__ void col_red_store(LAS float* red, int w, int lane, const f32x4 (&d)[4]) {
  if (lane < 32) {
    LAS float* p = red + w * 128 + (lane >> 4) * 64 + (lane & 15);
    p[0] = d[0].x; p[16] = d[1].x; p[32] = d[2].x; p[48] = d[3].x;
  }
}
__device__ __forceinline__ f32x4 col_red_sum_all(const LAS float* red, int lane) {
  const LAS f32x4* pr = (const LAS f32x4*)(red + (lane >> 5) * 4 * 128 + (lane & 31) * 4);
  f32x4 s = (pr[0] + pr[32]) + (pr[64] + pr[96]);
  const float a = fold32(s.x, s.y), b = fold32(s.z, s.w);
  auto sa = __builtin_amdgcn_permlane32_swap(f2u(a), f2u(a), false, false), sb = __builtin_amdgcn_permlane32_swap(f2u(b), f2u(b), false, false);
  const unsigned x = sa[0], y = sa[1], z = sb[0], ww = sb[1];
  return f32x4{u2f(x), u2f(y), u2f(z), u2f(ww)};
}
__device__ __forceinline__ f32x2 col_red_sum(const LAS float* red, int w, int lane) {
  f32x2 s = *(const LAS f32x2*)(red + (lane >> 3) * 128 + w * 16 + (lane & 7) * 2);
  s.x += dpp<ROR8>(s.x); s.y += dpp<ROR8>(s.y);
  s.x = sum16(s.x); s.y = sum16(s.y);
  s.x = sum32(s.x); s.y = sum32(s.y);
  return s;
}
__device__ __forceinline__ float silu_rb(float v) { return rb(v * rcp(1.f + ex2(-LOG2E * v))); }
__device__ __forceinline__ float ln_rden(float var) { return rcp(rb(__builtin_amdgcn_sqrtf(rb(rb(var) + 1e-5f)))); }
__device__ __forceinline__ unsigned make_tag(const LAS char* L) {
  const v2u e = *(const LAS v2u*)(L + L_EP);
  const u64 epoch = ((u64)__builtin_amdgcn_readfirstlane(e.y) << 32) | (unsigned)__builtin_amdgcn_readfirstlane(e.x);
  return (unsigned)(((epoch + TAGC) * TAGK) >> 32);
}

__global__ void __launch_bounds__(NTHR, 2) fused_kernel(Params p) {
  extern __shared__ __attribute__((aligned(16))) char smem[];
  LAS char* L = (LAS char*)smem;
  const u64 t0 = rt();
  const int tid = threadIdx.x, lane = tid & 63, w = __builtin_amdgcn_readfirstlane(tid >> 6), bid = blockIdx.x;
  const unsigned lbase = (unsigned)(size_t)L;
  const bool isX = bid < 128;
  const int jj = bid & 127;
  {
    const char* base = isX ? (const char*)(p.wmod + (jj >> 2) * 64) : (const char*)(p.wq + (jj >> 1) * 64);
    const int row0 = isX ? (jj & 3) * 256 + w * 32 : (jj & 1) * 512 + w * 64, stride = isX ? 12288 : 8192;
    const unsigned dst = lbase + (isX ? L_V + w * 4096 : w * 8192);
    const int r = lane >> 3;
    const char* src = base + (size_t)(row0 + r) * stride;
    const int c0 = ((lane & 7) ^ (((r >> 1) & 1) << 1)) * 16, c1 = c0 ^ 64;
    if (w == 0) dma16_keep(p.ws + WS_EPOCH + lane * 16, lbase + L_EP);
    if (PRETOUCH && w == 1) {
      const int ln = min(bid * 64 + lane, (WS_END - WS_E1) / 128 - 1);
      dma16_keep(p.ws + WS_E1 + (size_t)ln * 128, lbase + L_PF);
    }
    if (w >= 6) {
      u64 pc;
      asm volatile("s_getpc_b64 %0" : "=s"(pc));
      const int sl = (bid >> 3) & 15;
      if (sl < PF_SLICES) dma16_keep((const char*)(pc & ~255ull) + sl * 2048 + (w - 6) * 1024 + lane * 16, lbase + L_PF);
    }
    const char* s0 = isX ? (const char*)(p.c + ((lane >> 2) & 1) * 1024 + row0 + (lane & 3) * 8) : (const char*)(p.x + (w >> 2) * 1024 + lane * 8);
    const char* s1 = isX ? (const char*)(p.c + ((lane >> 3) & 1) * 1024 + (jj & 7) * 128 + (w & 1) * 64 + (lane & 7) * 8) : s0 + 1024;
    dma16(s0, lbase + L_XC + w * 2048);
    dma16(s1, lbase + L_XC + w * 2048 + 1024);
    if (!isX) wait_until<D_Y>(t0);
#pragma unroll
    for (int i = 0; i < 4; ++i) dma16(src + (size_t)i * 8 * stride + ((i & 1) ? c1 : c0), dst + i * 1024);
    if (!isX) {
#pragma unroll
      for (int i = 4; i < 8; ++i) dma16(src + (size_t)i * 8 * stride + ((i & 1) ? c1 : c0), dst + i * 1024);
    }
  }
  const rsrc_t rs = __builtin_amdgcn_make_buffer_rsrc((void*)p.ws, 0, WS_SIZE, 0x00020000);
  LAS float* red = (LAS float*)(L + L_RED);
  const int g = lane >> 4, c = lane & 15;
  if (w == 0) STAMP(0);

  if (__builtin_expect(isX, 1)) {
    const int j = jj, kq = j & 3, cg = j >> 2;
    const int b = j >> 6, hkv = (j >> 4) & 3, sp = j & 15;
    LAS char* act = L + L_ACT + w * 128;
    LAS char* lact = L + L_LA + w * 256;
    VMCNT(4);
    unsigned T = 0;
    {
      const unsigned craw = *(const LAS u16*)(L + L_XC + w * 2048 + lane * 2);
      const unsigned c2 = *(const LAS unsigned*)(L + L_XC + w * 2048 + 1024 + lane * 4);
      const float s = silu_rb(bflo(craw));
      *(LAS u16*)(act + lane * 2) = (u16)pk2(s, s);
      *(LAS unsigned*)(lact + lane * 4) = pk2(silu_rb(bflo(c2)), silu_rb(bfhi(c2)));
    }
    if (tid < 4) ((volatile LAS unsigned*)(L + L_PAIR))[tid] = 0u;
    if (w == 0) T = make_tag(L);
    asm volatile("s_waitcnt lgkmcnt(0)" ::: "memory");
    const v4u a1 = act_frag(act, 32, 0, lane);
    const int e1a = WS_E1 + ((kq * 2 + ((lane >> 4) & 1)) * 2048 + cg * 64 + (lane & 15) * 4) * 4;
    const unsigned tb1 = tagb(T, e1a);
    VMCNT(0);
    {
      f32x4 d[4];
#pragma unroll
      for (int ct = 0; ct < 4; ++ct) d[ct] = mfma(a1, col_frag(L + L_V + w * 4096, 0, ct, lane), f32x4{0.f, 0.f, 0.f, 0.f});
      col_red_store(red, w, lane, d);
    }
    if (w == 0) STAMP(1);
    BAR();
    if (w == 0) {
      const f32x4 s = col_red_sum_all(red, lane);
      if (lane < 32) st16(rs, e1a, v4u{tg(s.x, tb1), f2u(s.y), tg(s.z, tb1 >> 16), f2u(s.w)});
      STAMP(2);
    } else T = make_tag(L);
    BAR();
    {
      const size_t kvoff = ((size_t)(b * 4 + hkv) * 32768 + (32768 - 4096) + sp * 256 + w * 32) * 256;
      const char* ksrc = (const char*)p.kc + kvoff + g * 256;
      const char* vsrc = (const char*)p.vc + kvoff + g * 256;
      wait_until<D_K>(t0);
#pragma unroll
      for (int i = 0; i < 8; ++i) dma16(ksrc + i * 1024 + ((c ^ ((i * 4 + g) & 15)) * 16), lbase + L_K + w * 8192 + i * 1024);
#pragma unroll
      for (int i = 0; i < 8; ++i) dma16(vsrc + i * 1024 + ((c ^ (((i * 4 + g) & 7) << 1)) * 16), lbase + L_V + w * 8192 + i * 1024);
    }
    const LAS char* qp = L + L_QS + (c & 7) * 272 + g * 16;
    LAS float* mlw = (LAS float*)(L + L_ML) + (c & 7) * 8;
    const int e3oa = WS_E3 + (((b * 4 + hkv) * 4) * 16 + sp) * 1024 + w * 128 + (g & 1) * 64 + c * 4;
    const unsigned e3m = (c & 3) == 0 ? 0xFFFFFF00u : 0xFFFFFFFFu, e3t = (c & 3) == 0 ? (tagb(T, e3oa) & 0xFFu) : 0u;
    const int e3sa = WS_E3S + ((b * 4 + hkv) * 16 + sp) * 128 + c * 8;
    const unsigned tb3s = tagb(T, e3sa);
    const int qa = WS_E2 + (((lane >> 5) * 2 + b) * 4096 + hkv * 1024 + w * 128 + (lane & 31) * 4) * 4;
    const unsigned tq0 = tagb(T, qa) & 0xFFu, tq2 = (tagb(T, qa) >> 16) & 0xFFu;
    LAS char* qd = L + L_QS + w * 272 + ((lane & 31) * 4 + (lane >> 5)) * 2;
    const u16* lsrc = p.wmod + 2048 + (w >> 1) * 1024 + (j >> 3) * 64;
    const int lrow = (j & 7) * 128 + (w & 1) * 64;
    VMCNT(8);
    if (w == 0) STAMP(5);
    v4u kf[8];
#pragma unroll
    for (int Tt = 0; Tt < 2; ++Tt)
#pragma unroll
      for (int s = 0; s < 4; ++s) kf[Tt * 4 + s] = *(const LAS v4u*)(L + L_K + w * 8192 + (Tt * 16 + c) * 256 + (((s * 4 + g) ^ c) * 16));
#pragma unroll
    for (int i = 0; i < 8; ++i) asm volatile("" : "+v"(kf[i]));
    asm volatile("s_waitcnt vmcnt(0) lgkmcnt(0)" ::: "memory");
    if (w == 0) STAMP(6);
    BAR();
    v4u vf[8];
    {
      const int q = c >> 2, pp = c & 3, r0 = 4 * g + q;
      const LAS char* vb = L + L_V + r0 * 256 + ((w ^ (r0 & 7)) * 32) + pp * 8;
#pragma unroll
      for (int ks = 0; ks < 8; ++ks) vf[ks] = tr_frag(vb + ks * 8192, vb + ks * 8192 + 4096);
    }
    if (w < LATE_W) dma_col<8>(lsrc, lrow, 12288, lbase + L_K + w * 8192, lane);
#pragma unroll
    for (int i = 0; i < 8; ++i) asm volatile("" : "+v"(vf[i]));
    {
      v4u v;
      for (int spin = 0; spin < SPIN_CAP; ++spin) {
        FENCE();
        v = ld16(rs, qa);
        unsigned acc = 0u;
        v.x = strip(acc, v.x, tq0); v.z = strip(acc, v.z, tq2);
        if (allok(acc)) break;
      }
      const unsigned pq = pk2(fold32(u2f(v.x), u2f(v.y)), fold32(u2f(v.z), u2f(v.w)));
      *(LAS u16*)qd = (u16)pq; *(LAS u16*)(qd + 4) = (u16)(pq >> 16);
      if (w == 0) STAMP(3);
    }
    BAR();
    if (w == 0) STAMP(4);
    f32x4 sc[2];
    {
      v4u qf[4];
#pragma unroll
      for (int s = 0; s < 4; ++s) qf[s] = *(const LAS v4u*)(qp + s * 64);
      SB();
      f32x4 d0 = mfma(kf[0], qf[0], f32x4{0.f, 0.f, 0.f, 0.f}), d1 = mfma(kf[4], qf[0], f32x4{0.f, 0.f, 0.f, 0.f});
#pragma unroll
      for (int s = 1; s < 4; ++s) { d0 = mfma(kf[s], qf[s], d0); d1 = mfma(kf[4 + s], qf[s], d1); }
      sc[0] = d0; sc[1] = d1;
    }
    {
      const f32x2 sca = {0.08838834764831845f, 0.08838834764831845f};
#pragma unroll
      for (int Tt = 0; Tt < 2; ++Tt) {
        const f32x2 lo = rb2(rb2(f32x2{sc[Tt].x, sc[Tt].y}) * sca), hi = rb2(rb2(f32x2{sc[Tt].z, sc[Tt].w}) * sca);
        sc[Tt] = f32x4{lo.x, lo.y, hi.x, hi.y};
      }
      float mx = vmax(vmax3(vmax3(sc[0].x, sc[0].y, sc[0].z), sc[0].w, sc[1].x), vmax3(sc[1].y, sc[1].z, sc[1].w));
      mx = max32(max16(mx));
      if (lane < 8) mlw[w] = mx;
    }
    if (w == 0) STAMP(15);
    BAR();
    float M;
    {
      const f32x4 m0 = *(const LAS f32x4*)mlw, m1 = *(const LAS f32x4*)(mlw + 4);
      M = vmax(vmax3(vmax3(m0.x, m0.y, m0.z), m0.w, m1.x), vmax3(m1.y, m1.z, m1.w));
      const float nm = -M * LOG2E;
#pragma unroll
      for (int Tt = 0; Tt < 2; ++Tt)
#pragma unroll
        for (int r = 0; r < 4; ++r) sc[Tt][r] = ex2(fmaf(sc[Tt][r], LOG2E, nm));
      *(LAS v4u*)(L + L_PA + w * 1024 + lane * 16) = v4u{pk2(sc[0].x, sc[0].y), pk2(sc[0].z, sc[0].w), pk2(sc[1].x, sc[1].y), pk2(sc[1].z, sc[1].w)};
      const f32x4 t4 = sc[0] + sc[1];
      const float ls = sum32(sum16((t4.x + t4.y) + (t4.z + t4.w)));
      if (lane < 8) mlw[64 + w] = ls;
    }
    BAR();
    if (w == 0) STAMP(14);
    {
      v4u pa[8];
#pragma unroll
      for (int ks = 0; ks < 8; ++ks) pa[ks] = *(const LAS v4u*)(L + L_PA + ks * 1024 + lane * 16);
      SB();
      f32x4 o0 = mfma(pa[0], vf[0], f32x4{0.f, 0.f, 0.f, 0.f}), o1 = mfma(pa[1], vf[1], f32x4{0.f, 0.f, 0.f, 0.f});
#pragma unroll
      for (int ks = 2; ks < 8; ks += 2) { o0 = mfma(pa[ks], vf[ks], o0); o1 = mfma(pa[ks + 1], vf[ks + 1], o1); }
      const f32x4 o = o0 + o1;
      if (lane < 32) {
#pragma unroll
        for (int r = 0; r < 4; ++r) st4(rs, e3oa + r * 16384, (f2u(o[r]) & e3m) | e3t);
      }
      if (w == 7) {
        const f32x4 l4 = *(const LAS f32x4*)(mlw + 64) + *(const LAS f32x4*)(mlw + 68);
        if (lane < 16) st8(rs, e3sa, v2u{tg(M, tb3s), tg((l4.x + l4.y) + (l4.z + l4.w), tb3s >> 8)});
      }
      if (w == 0) STAMP(7);
    }
    {
      const int ela = WS_EL + (((w >> 1) * 8 + (j & 7)) * 2 + ((lane >> 4) & 1)) * 4096 + ((j >> 3) * 64 + (lane & 15) * 4) * 4;
      const unsigned tl = tagb(T, ela);
      const v4u a0 = act_frag(lact, 64, 0, lane), a1_ = act_frag(lact, 64, 32, lane);
      BAR();
      if (w >= LATE_W) {
        dma_col<8>(lsrc, lrow, 12288, lbase + L_K + w * 8192, lane);
        VMCNT(0);
      }
      v4u bfr[8];
#pragma unroll
      for (int ks = 0; ks < 2; ++ks)
#pragma unroll
        for (int ct = 0; ct < 4; ++ct) bfr[ks * 4 + ct] = col_frag(L + L_K + w * 8192, ks, ct, lane);
      f32x4 d[4];
#pragma unroll
      for (int ct = 0; ct < 4; ++ct) d[ct] = mfma(a1_, bfr[4 + ct], mfma(a0, bfr[ct], f32x4{0.f, 0.f, 0.f, 0.f}));
      volatile LAS unsigned* pf = (volatile LAS unsigned*)(L + L_PAIR) + (w >> 1);
      if (w & 1) {
        col_red_store(red, w, lane, d);
        asm volatile("s_waitcnt lgkmcnt(0)" ::: "memory");
        if (lane == 0) *pf = 1u;
      } else {
        col_red_store(red, w, lane, d);
        for (int spin = 0; spin < (1 << 22) && *pf == 0u; ++spin) {}
        asm volatile("s_waitcnt lgkmcnt(0)" ::: "memory");
        if (lane < 32) {
          const LAS f32x4* pr = (const LAS f32x4*)(red + w * 128 + lane * 4);
          const f32x4 s = pr[0] + pr[32];
          st16(rs, ela, v4u{tg(s.x, tl), f2u(s.y), f2u(s.z), f2u(s.w)});
        }
        if (w == 0) STAMP(8);
      }
    }
  } else {
    const int j = jj, kh = j & 1, cg = j >> 1;
    unsigned T;
    const char* nsrc = (j < 64 ? (const char*)p.wo + (size_t)((j >> 3) * 512 + (j & 7) * 16 + g) * 2048 : (const char*)p.w2 + (size_t)((j - 64) * 64 + g) * 2048) + w * 256;
    {
      const int eb = tid >> 8, ek = (tid & 255) * 2;
      const int e1o = WS_E1 + (eb * 2048 + kh * 512 + ek) * 4;
      const int e2a = WS_E2 + ((kh * 2 + ((lane >> 4) & 1)) * 4096 + cg * 64 + (lane & 15) * 4) * 4;
      LAS char* act = L + L_ACT;
      f32x2 xn;
      VMCNT(8);
      {
        const v4u xa = *(const LAS v4u*)(L + L_XC + w * 2048 + lane * 32), xb = *(const LAS v4u*)(L + L_XC + w * 2048 + lane * 32 + 16);
        const unsigned own = *(const LAS unsigned*)(L + L_XC + w * 2048 + (kh * 512 + ek) * 2);
        float v[16];
#pragma unroll
        for (int i = 0; i < 4; ++i) { v[2 * i] = bflo(xa[i]); v[2 * i + 1] = bfhi(xa[i]); v[8 + 2 * i] = bflo(xb[i]); v[9 + 2 * i] = bfhi(xb[i]); }
        float s = 0.f;
#pragma unroll
        for (int i = 0; i < 16; ++i) s += v[i];
        s += dpp<XOR1>(s); s += dpp<XOR2>(s); s += dpp<ROR4>(s); s += dpp<ROR8>(s); s = sum32(sum16(s));
        const float mean = s * (1.f / 1024.f);
        float q = 0.f;
#pragma unroll
        for (int i = 0; i < 16; ++i) { const float d = v[i] - mean; q += d * d; }
        q += dpp<XOR1>(q); q += dpp<XOR2>(q); q += dpp<ROR4>(q); q += dpp<ROR8>(q); q = sum32(sum16(q));
        const float mb = rb(mean), rd = ln_rden(q * (1.f / 1024.f));
        xn = rb2(rb2(up2(own) - mb) * rd);
      }
      VMCNT(0);
      v4u bf[8];
#pragma unroll
      for (int ks = 0; ks < 2; ++ks)
#pragma unroll
        for (int ct = 0; ct < 4; ++ct) bf[ks * 4 + ct] = col_frag(L + w * 8192, ks, ct, lane);
#pragma unroll
      for (int i = 0; i < 8; ++i) asm volatile("" : "+v"(bf[i]));
      BAR();
      T = make_tag(L);
      const unsigned t1 = tagb(T, e1o) & 0xFFu, t2 = tagb(T, e2a);
      if (j < 120) {
#pragma unroll
        for (int i = 0; i < EARLY_W; ++i) dma16(nsrc + next_row(j, i) * 2048 + row_src_off(i, lane), lbase + w * 16384 + i * 1024);
      }
      if (w == 0) STAMP(1);
      wait_until<D_PE1>(t0);
      {
        v2u a[4], d[4];
        for (int spin = 0; spin < SPIN_CAP; ++spin) {
          FENCE();
#pragma unroll
          for (int q = 0; q < 4; ++q) { a[q] = ld8(rs, e1o + q * 16384); d[q] = ld8(rs, e1o + q * 16384 + 4096); }
          unsigned acc = 0u;
#pragma unroll
          for (int q = 0; q < 4; ++q) { a[q].x = strip(acc, a[q].x, t1); d[q].x = strip(acc, d[q].x, t1); }
          if (allok(acc)) break;
        }
        const f32x2 s = (asf2(a[0]) + asf2(a[1])) + (asf2(a[2]) + asf2(a[3]));
        const f32x2 cc = (asf2(d[0]) + asf2(d[1])) + (asf2(d[2]) + asf2(d[3]));
        const f32x2 h = rb2(xn * rb2(rb2(cc) + 1.f)) + rb2(s);
        *(LAS unsigned*)(act + (eb * 512 + ek) * 2) = pk2(h);
        if (w == 0) STAMP(2);
      }
      BAR();
      if (w == 0) STAMP(9);
      {
        const v4u a0 = act_frag(act, 512, w * 64, lane), a1 = act_frag(act, 512, w * 64 + 32, lane);
        f32x4 d[4];
#pragma unroll
        for (int ct = 0; ct < 4; ++ct) d[ct] = mfma(a1, bf[4 + ct], mfma(a0, bf[ct], f32x4{0.f, 0.f, 0.f, 0.f}));
        col_red_store(red, w, lane, d);
      }
      BAR();
      if (w == 0) {
        const f32x4 s = col_red_sum_all(red, lane);
        if (lane < 32) st16(rs, e2a, v4u{tg(s.x, t2), f2u(s.y), tg(s.z, t2 >> 16), f2u(s.w)});
        STAMP(3);
      }
      BAR();
    }
    if (j < 64) {
      const int hq = j >> 3, d16 = j & 7;
      v4u wf[16];
      wait_until<D_WO>(t0);
#pragma unroll
      for (int i = EARLY_W; i < 16; ++i) dma16(nsrc + (size_t)((i >> 2) * 128 + (i & 3) * 4) * 2048 + row_src_off(i, lane), lbase + w * 16384 + i * 1024);
      const int bb = w >> 2, hh = w & 3;
      const int oo = WS_E3 + (((bb * 4 + (hq >> 1)) * 4 + hh) * 16 + (lane >> 2)) * 1024 + d16 * 128 + (hq & 1) * 64 + (lane & 3) * 16;
      const int so = WS_E3S + ((bb * 4 + (hq >> 1)) * 16 + (lane >> 2)) * 128 + ((hq & 1) * 4 + hh) * 8;
      const unsigned to = tagb(T, oo) & 0xFFu, ts0 = tagb(T, so) & 0xFFu, ts1 = (tagb(T, so) >> 8) & 0xFFu;
      const int e4a = WS_E4 + (j * 64 + 8 * w) * 128 + (g & 1) * 64 + c * 4;
      const unsigned e4m = (c & 3) == 0 ? 0xFFFFFF00u : 0xFFFFFFFFu, e4t = (c & 3) == 0 ? (tagb(T, e4a) & 0xFFu) : 0u;
      const int p8 = lane >> 3, ch = lane & 7, rbb = ch >> 2, col = j * 16 + (ch & 3) * 4 + comp4(lane);
      const int elo = WS_EL + (p8 * 2 + rbb) * 4096 + (j * 16 + (ch & 3) * 4) * 4;
      const int e4o = WS_E4 + (p8 * 64 + j) * 128 + ch * 16, e6o = WS_E6 + (p8 * 64 + j) * 128 + ch * 16;
      const int e5a = WS_E5 + j * 128 + ch * 16 + comp4(lane) * 4;
      const unsigned tel = tagb(T, elo) & 0xFFu, t4 = tagb(T, e4o) & 0xFFu, t6 = tagb(T, e6o) & 0xFFu, t5 = tagb(T, e5a);
      float xv = 0.f, b2v = 0.f;
      if (w == 0) { xv = bflo(p.x[rbb * 1024 + col]); b2v = bflo(p.b2[col]); }
      VMCNT(0);
#pragma unroll
      for (int ks = 0; ks < 2; ++ks)
#pragma unroll
        for (int ct = 0; ct < 8; ++ct) wf[ks * 8 + ct] = row_frag(L + w * 16384, ks, ct, lane);
#pragma unroll
      for (int i = 0; i < 16; ++i) asm volatile("" : "+v"(wf[i]));
      if (w == 0) STAMP(4);
      {
        v4u o4; v2u s2;
        for (int spin = 0; spin < SPIN_CAP; ++spin) {
          FENCE();
          o4 = ld16(rs, oo); s2 = ld8(rs, so);
          unsigned acc = 0u;
          o4.x = strip(acc, o4.x, to); s2.x = strip(acc, s2.x, ts0); s2.y = strip(acc, s2.y, ts1);
          if (allok(acc)) break;
        }
        if (w == 0) STAMP(5);
        const float m = u2f(s2.x);
        float Mx = vmax(m, dpp<ROR4>(m));
        Mx = vmax(Mx, dpp<ROR8>(Mx)); Mx = max32(max16(Mx));
        const float f = ex2((m - Mx) * LOG2E);
        f32x4 o = f * asf(o4);
        float l = f * u2f(s2.y);
        o.x += dpp<ROR4>(o.x); o.y += dpp<ROR4>(o.y); o.z += dpp<ROR4>(o.z); o.w += dpp<ROR4>(o.w); l += dpp<ROR4>(l);
        l += dpp<ROR8>(l); l = sum32(sum16(l));
        const float ov = red4_345(o) * rcp(l);
        if ((lane & 12) == 0) *(LAS u16*)(L + L_OA + (bb * 64 + hh * 16 + (lane & 3) * 4 + comp4(lane)) * 2) = (u16)pk2(ov, ov);
      }
      BAR();
      if (w == 0) STAMP(6);
      {
        const v4u a0 = act_frag(L + L_OA, 64, 0, lane), a1 = act_frag(L + L_OA, 64, 32, lane);
        f32x4 d[8];
#pragma unroll
        for (int ct = 0; ct < 8; ++ct) d[ct] = mfma(a0, wf[ct], f32x4{0.f, 0.f, 0.f, 0.f});
#pragma unroll
        for (int ct = 0; ct < 8; ++ct) d[ct] = mfma(a1, wf[8 + ct], d[ct]);
        if (lane < 32) {
#pragma unroll
          for (int ct = 0; ct < 8; ++ct) st4(rs, e4a + ct * 128, (f2u(d[ct].x) & e4m) | e4t);
        }
        if (w == 0) STAMP(7);
      }
      if (w == 0) {
        float at, gav;
        {
          v4u a[8], e;
          for (int spin = 0; spin < SPIN_CAP; ++spin) {
            FENCE();
#pragma unroll
            for (int i = 0; i < 8; ++i) a[i] = ld16(rs, e4o + i * 65536);
            e = ld16(rs, elo);
            unsigned acc = 0u;
#pragma unroll
            for (int i = 0; i < 8; ++i) a[i].x = strip(acc, a[i].x, t4);
            e.x = strip(acc, e.x, tel);
            if (allok(acc)) break;
          }
          at = red4_345(((asf(a[0]) + asf(a[1])) + (asf(a[2]) + asf(a[3]))) + ((asf(a[4]) + asf(a[5])) + (asf(a[6]) + asf(a[7]))));
          gav = rb(red4_345(asf(e)));
        }
        STAMP(10);
        const float x2 = rb(xv + rb(gav * rb(at)));
        if ((lane & 8) == 0) st4(rs, e5a, tg(x2, t5));
        STAMP(11);
        {
          v4u a[2];
          for (int spin = 0; spin < SPIN_CAP; ++spin) {
            FENCE();
            a[0] = ld16(rs, elo + 65536); a[1] = ld16(rs, elo + 2 * 65536);
            unsigned acc = 0u;
            a[0].x = strip(acc, a[0].x, tel); a[1].x = strip(acc, a[1].x, tel);
            if (allok(acc)) break;
          }
          const float sbv = rb(red4_345(asf(a[0]))), cbv = rb(1.f + rb(red4_345(asf(a[1]))));
          if ((lane & 8) == 0) { st4(rs, e5a + 8192, tg(sbv, t5)); st4(rs, e5a + 16384, tg(cbv, t5)); }
        }
        float gbv;
        {
          v4u e;
          for (int spin = 0; spin < SPIN_CAP; ++spin) {
            FENCE();
            e = ld16(rs, elo + 3 * 65536);
            unsigned acc = 0u;
            e.x = strip(acc, e.x, tel);
            if (allok(acc)) break;
          }
          gbv = rb(red4_345(asf(e)));
        }
        float mo;
        {
          v4u a[7];
          for (int spin = 0; spin < SPIN_CAP; ++spin) {
            FENCE();
#pragma unroll
            for (int i = 0; i < 7; ++i) a[i] = ld16(rs, e6o + i * 65536);
            unsigned acc = 0u;
#pragma unroll
            for (int i = 0; i < 7; ++i) a[i].x = strip(acc, a[i].x, t6);
            if (allok(acc)) break;
          }
          mo = red4_345(((asf(a[0]) + asf(a[1])) + (asf(a[2]) + asf(a[3]))) + ((asf(a[4]) + asf(a[5])) + asf(a[6])));
        }
        const float ov = x2 + rb(gbv * rb(rb(mo) + b2v));
        if ((lane & 8) == 0) ((GAS u16*)p.out)[rbb * 1024 + col] = (u16)pk2(ov, ov);
        if (j == 0 && lane == 0) {
          const v2u e = *(const LAS v2u*)(L + L_EP);
          const u64 e1 = (((u64)e.y << 32) | e.x) + 1;
          st8(rs, WS_EPOCH, v2u{(unsigned)e1, (unsigned)(e1 >> 32)});
        }
        STAMP(12);
      }
    } else if (j < 120) {
      const int m = j - 64;
      const unsigned b1r = *(const GAS unsigned*)(p.b1 + m * 64 + (w & 3) * 16 + (lane & 7) * 2);
      const int bb = (tid >> 2) & 1, k0 = (tid >> 3) * 16 + (tid & 3) * 4;
      const int e5o = WS_E5 + tid * 16;
      v4u w1f[16], w2f[16];
      {
        const LAS char* img = L + w * 16384;
        const unsigned dst = lbase + w * 16384;
        wait_until<D_M>(t0);
#pragma unroll
        for (int i = EARLY_W; i < 16; ++i) dma16(nsrc + (size_t)i * 4 * 2048 + row_src_off(i, lane), dst + i * 1024);
        VMCNT(8);
#pragma unroll
        for (int ct = 0; ct < 8; ++ct) w2f[ct] = row_frag(img, 0, ct, lane);
#pragma unroll
        for (int ct = 0; ct < 8; ++ct) asm volatile("" : "+v"(w2f[ct]));
        asm volatile("s_waitcnt lgkmcnt(0)" ::: "memory");
        dma_col<8>(p.w1 + m * 64, w * 128, 7168, dst, lane);
        VMCNT(8);
#pragma unroll
        for (int ct = 0; ct < 8; ++ct) w2f[8 + ct] = row_frag(img, 1, ct, lane);
#pragma unroll
        for (int ct = 0; ct < 8; ++ct) asm volatile("" : "+v"(w2f[8 + ct]));
        asm volatile("s_waitcnt lgkmcnt(0)" ::: "memory");
        dma_col<8>(p.w1 + m * 64, w * 128 + 64, 7168, dst + 8192, lane);
        VMCNT(0);
#pragma unroll
        for (int ks = 0; ks < 4; ++ks)
#pragma unroll
          for (int ct = 0; ct < 4; ++ct) w1f[ks * 4 + ct] = col_frag(img, ks, ct, lane);
#pragma unroll
        for (int i = 0; i < 16; ++i) asm volatile("" : "+v"(w1f[i]));
        if (w == 0) STAMP(12);
      }
      const int e6a = WS_E6 + (m * 64 + 8 * w) * 128 + (g & 1) * 64 + c * 4;
      const unsigned e6m = (c & 3) == 0 ? 0xFFFFFF00u : 0xFFFFFFFFu, e6t = (c & 3) == 0 ? (tagb(T, e6a) & 0xFFu) : 0u;
      const unsigned t5 = tagb(T, e5o);
      LAS float* lns = (LAS float*)(L + L_LN);
      LAS float* lnw = lns + (bb * 2 + (lane >> 4)) * 8 + w;
      const LAS f32x4* lnr = (const LAS f32x4*)(lns + bb * 16);
      f32x2 v0, v1;
      {
        v4u xv;
        for (int spin = 0; spin < SPIN_CAP; ++spin) {
          FENCE();
          xv = ld16(rs, e5o);
          if (allok(chk(chk(chk(chk(0u, xv.x, t5), xv.y, t5 >> 8), xv.z, t5 >> 16), xv.w, t5 >> 24))) break;
        }
        v0 = f32x2{bfhi(xv.x), bfhi(xv.y)}; v1 = f32x2{bfhi(xv.z), bfhi(xv.w)};
      }
      v4u sbr = ld16(rs, e5o + 8192), cbr = ld16(rs, e5o + 16384);
      if (w == 0) STAMP(5);
      float mb, rd;
      auto stats = [&](int stage) {
        const f32x2 s2 = v0 + v1, q2 = v0 * v0 + v1 * v1;
        float s = s2.x + s2.y, q = q2.x + q2.y;
        s += dpp<XOR1>(s); q += dpp<XOR1>(q);
        s += dpp<XOR2>(s); q += dpp<XOR2>(q);
        s += dpp<ROR8>(s); q += dpp<ROR8>(q);
        const float t = sum32(fold16(s, q));
        if ((lane & ~20) == 0) lnw[stage * 32] = t;
        BAR();
        const f32x4 a = lnr[stage * 8] + lnr[stage * 8 + 1], e = lnr[stage * 8 + 2] + lnr[stage * 8 + 3];
        const float mean = ((a.x + a.y) + (a.z + a.w)) * (1.f / 1024.f);
        const float var = fmaf(-mean, mean, ((e.x + e.y) + (e.z + e.w)) * (1.f / 1024.f));
        mb = rb(mean); rd = ln_rden(fmaxf(var, 0.f));
      };
      stats(0);
      if (w == 0) STAMP(13);
      f32x2 sb0, sb1, cb0, cb1;
      {
        for (int spin = 0; spin < SPIN_CAP; ++spin) {
          if (allok(chk(chk(chk(chk(chk(chk(chk(chk(0u, sbr.x, t5), sbr.y, t5 >> 8), sbr.z, t5 >> 16), sbr.w, t5 >> 24), cbr.x, t5), cbr.y, t5 >> 8), cbr.z, t5 >> 16), cbr.w, t5 >> 24))) break;
          FENCE();
          sbr = ld16(rs, e5o + 8192); cbr = ld16(rs, e5o + 16384);
        }
        sb0 = f32x2{bfhi(sbr.x), bfhi(sbr.y)}; sb1 = f32x2{bfhi(sbr.z), bfhi(sbr.w)};
        cb0 = f32x2{bfhi(cbr.x), bfhi(cbr.y)}; cb1 = f32x2{bfhi(cbr.z), bfhi(cbr.w)};
      }
      v0 = rb2(rb2(rb2(rb2(v0 - mb) * rd) * cb0) + sb0);
      v1 = rb2(rb2(rb2(rb2(v1 - mb) * rd) * cb1) + sb1);
      stats(1);
      if (w == 0) STAMP(14);
      *(LAS v2u*)(L + L_ACT + (bb * 1024 + k0) * 2) = v2u{pk2(rb2(v0 - mb) * rd), pk2(rb2(v1 - mb) * rd)};
      BAR();
      if (w == 0) STAMP(6);
      {
        v4u a[4];
#pragma unroll
        for (int ks = 0; ks < 4; ++ks) a[ks] = act_frag(L + L_ACT, 1024, w * 128 + ks * 32, lane);
        f32x4 d[4];
#pragma unroll
        for (int ct = 0; ct < 4; ++ct) d[ct] = mfma(a[0], w1f[ct], f32x4{0.f, 0.f, 0.f, 0.f});
#pragma unroll
        for (int ks = 1; ks < 4; ++ks)
#pragma unroll
          for (int ct = 0; ct < 4; ++ct) d[ct] = mfma(a[ks], w1f[ks * 4 + ct], d[ct]);
        col_red_store(red, w, lane, d);
      }
      BAR();
      if (w == 0) STAMP(15);
      {
        const f32x2 z = rb2(rb2(col_red_sum(red, w, lane)) + up2(b1r));
        const f32x2 u = (z + 0.044715f * z * z * z) * (-2.f * 0.7978845608028654f * LOG2E);
        const float g0 = z.x * rcp(1.f + ex2(u.x)), g1 = z.y * rcp(1.f + ex2(u.y));
        if (lane < 8) *(LAS unsigned*)(L + L_OA + ((w >> 2) * 64 + (w & 3) * 16 + lane * 2) * 2) = pk2(g0, g1);
      }
      BAR();
      if (w == 0) STAMP(7);
      {
        const v4u a0 = act_frag(L + L_OA, 64, 0, lane), a1 = act_frag(L + L_OA, 64, 32, lane);
        f32x4 d[8];
#pragma unroll
        for (int ct = 0; ct < 8; ++ct) d[ct] = mfma(a0, w2f[ct], f32x4{0.f, 0.f, 0.f, 0.f});
#pragma unroll
        for (int ct = 0; ct < 8; ++ct) d[ct] = mfma(a1, w2f[8 + ct], d[ct]);
        if (lane < 32) {
#pragma unroll
          for (int ct = 0; ct < 8; ++ct) st4(rs, e6a + ct * 128, (f2u(d[ct].x) & e6m) | e6t);
        }
        if (w == 0) STAMP(8);
      }
    }
  }
  if (w == 0) STAMP_FLUSH();
}

}

extern "C" void kernel_launch(void* const* d_in, const int* in_sizes, int n_in, void* d_out, int out_size, void* d_ws, size_t ws_size,
                              hipStream_t stream) {
  Params p;
  p.x = (const u16*)d_in[0]; p.kc = (const u16*)d_in[1]; p.vc = (const u16*)d_in[2]; p.wq = (const u16*)d_in[3]; p.wo = (const u16*)d_in[4];
  p.w1 = (const u16*)d_in[6]; p.b1 = (const u16*)d_in[7]; p.w2 = (const u16*)d_in[8]; p.b2 = (const u16*)d_in[9];
  p.c = (const u16*)d_in[10]; p.wmod = (const u16*)d_in[11];
  p.out = (u16*)d_out; p.ws = (unsigned char*)d_ws;
  (void)hipFuncSetAttribute((const void*)fused_kernel, hipFuncAttributeMaxDynamicSharedMemorySize, LDS_BYTES);
  fused_kernel<<<256, NTHR, LDS_BYTES, stream>>>(p);
}
```
